```python
import torch

def _softmax(x, axis=-1):
    return torch.softmax(x, dim=axis)
def _gelu(x):
    return torch.nn.functional.gelu(x, approximate="tanh")
def _silu(x):
    return torch.nn.functional.silu(x)
def _sigmoid(x):
    return torch.sigmoid(x)
def _layernorm(x, axis=-1, eps=1e-5):
    m = x.mean(dim=axis, keepdim=True)
    v = x.var(dim=axis, keepdim=True, unbiased=False)
    return (x - m) / torch.sqrt(v + eps)
def _scatter_add(tgt, idx, src, axis=0):
    return tgt.clone().index_add_(axis, idx, src)
def _segment_sum(src, seg, n):
    out = torch.zeros((n,) + src.shape[1:], dtype=src.dtype)
    return out.index_add_(0, seg, src)
def _pad(x, pw):
    flat = tuple(v for pair in reversed(pw) for v in pair)
    return torch.nn.functional.pad(x, flat)

kernel_name = 'standalone__gqa_block_cached__near_ridge'

def setup_inputs():

    _w = (lambda a: (lambda t: t.to(torch.bfloat16) if t.is_floating_point() else t)(torch.as_tensor(a)))
    B, Sq, D = 16, 80, 448
    Hq, Hkv, Sk, Dh = 16, 8, 512, 256
    assert Hq % Hkv == 0, (Hq, Hkv)
    return {
        "x": _w((lambda *s: torch.randn(*s, dtype=torch.bfloat16) * 0.8)(B, Sq, D)),
        "k_cache": _w((lambda *s: torch.randn(*s, dtype=torch.bfloat16) * 0.8)(B, Hkv, Sk, Dh)),
        "v_cache": _w((lambda *s: torch.randn(*s, dtype=torch.bfloat16) * 0.05)(B, Hkv, Sk, Dh)),
        "wq": _w((lambda *s: torch.randn(*s, dtype=torch.bfloat16) * 0.8)(D, Hq * Dh) * (D ** -0.5)),
        "wo": _w((lambda *s: torch.randn(*s, dtype=torch.bfloat16) * 0.05)(Hq * Dh, D) * ((Hq * Dh) ** -0.5)),
    }

def reference(x, k_cache, v_cache, wq, wo):
    B, Sq, D = x.shape
    Hq, Dh = 16, 256
    Hkv = k_cache.shape[1]
    rep = Hq // Hkv
    q = (lambda x, *d: x.permute(*d))((x @ wq).reshape(B, Sq, Hq, Dh), 0, 2, 1, 3)
    k = (lambda x, n, axis: torch.repeat_interleave(x, n, dim=axis))(k_cache, rep, axis=1)
    v = (lambda x, n, axis: torch.repeat_interleave(x, n, dim=axis))(v_cache, rep, axis=1)
    scores = torch.einsum("bhqd,bhkd->bhqk", q, k) * (Dh ** -0.5)
    p = _softmax(scores, axis=-1)
    o = torch.einsum("bhqk,bhkd->bhqd", p, v)
    return (lambda x, *d: x.permute(*d))(o, 0, 2, 1, 3).reshape(B, Sq, Hq * Dh) @ wo
```

```cpp
#include <hip/hip_runtime.h>
#include <hip/hip_bf16.h>

constexpr int B = 16, SQ = 80, D = 448, HQ = 16, HKV = 8, SK = 512, DH = 256;
typedef unsigned short u16;
__device__ __forceinline__ float bf2f(u16 x) { return __uint_as_float(((unsigned)x) << 16); }
__device__ __forceinline__ u16 f2bf(float f) { __hip_bfloat16 h = __float2bfloat16(f); return *reinterpret_cast<u16*>(&h); }

__global__ void gemm_naive(const u16* A, const u16* Bm, u16* C, int M, int N, int K) {
  int n = blockIdx.x * blockDim.x + threadIdx.x;
  int m = blockIdx.y;
  if (n >= N) return;
  float acc = 0.f;
  for (int k = 0; k < K; ++k) acc += bf2f(A[(size_t)m * K + k]) * bf2f(Bm[(size_t)k * N + n]);
  C[(size_t)m * N + n] = f2bf(acc);
}

__global__ void attn_naive(const u16* Q, const u16* Kc, const u16* Vc, u16* O) {
  int s = blockIdx.x, h = blockIdx.y, b = blockIdx.z, t = threadIdx.x;
  __shared__ float q[DH];
  __shared__ float p[SK];
  __shared__ float red[256];
  q[t] = bf2f(Q[((size_t)(b * SQ + s)) * (HQ * DH) + h * DH + t]);
  __syncthreads();
  const u16* Kb = Kc + (size_t)(b * HKV + h / 2) * SK * DH;
  const u16* Vb = Vc + (size_t)(b * HKV + h / 2) * SK * DH;
  float mx = -1e30f;
  for (int j = t; j < SK; j += 256) {
    float acc = 0.f;
    for (int d = 0; d < DH; ++d) acc += q[d] * bf2f(Kb[(size_t)j * DH + d]);
    acc *= 0.0625f;
    p[j] = acc;
    mx = fmaxf(mx, acc);
  }
  red[t] = mx; __syncthreads();
  for (int o = 128; o > 0; o >>= 1) { if (t < o) red[t] = fmaxf(red[t], red[t + o]); __syncthreads(); }
  mx = red[0]; __syncthreads();
  float sum = 0.f;
  for (int j = t; j < SK; j += 256) { float e = __expf(p[j] - mx); p[j] = e; sum += e; }
  red[t] = sum; __syncthreads();
  for (int o = 128; o > 0; o >>= 1) { if (t < o) red[t] += red[t + o]; __syncthreads(); }
  sum = red[0];
  float acc = 0.f;
  for (int j = 0; j < SK; ++j) acc += p[j] * bf2f(Vb[(size_t)j * DH + t]);
  O[((size_t)(b * SQ + s)) * (HQ * DH) + h * DH + t] = f2bf(acc / sum);
}

extern "C" void kernel_launch(void* const* d_in, const int* in_sizes, int n_in, void* d_out, int out_size, void* d_ws, size_t ws_size, hipStream_t stream) {
  const u16* x = (const u16*)d_in[0];
  const u16* kc = (const u16*)d_in[1];
  const u16* vc = (const u16*)d_in[2];
  const u16* wq = (const u16*)d_in[3];
  const u16* wo = (const u16*)d_in[4];
  u16* Q = (u16*)d_ws;
  u16* O = Q + (size_t)B * SQ * HQ * DH;
  gemm_naive<<<dim3(HQ * DH / 256, B * SQ), 256, 0, stream>>>(x, wq, Q, B * SQ, HQ * DH, D);
  attn_naive<<<dim3(SQ, HQ, B), 256, 0, stream>>>(Q, kc, vc, O);
  gemm_naive<<<dim3(2, B * SQ), 256, 0, stream>>>(O, wo, (u16*)d_out, B * SQ, D, HQ * DH);
}
```

```cpp
#include <hip/hip_runtime.h>
#include <hip/hip_bf16.h>

typedef __bf16 bf16x2 __attribute__((ext_vector_type(2)));
typedef float f32x2 __attribute__((ext_vector_type(2)));
typedef unsigned u32x4 __attribute__((ext_vector_type(4)));
typedef unsigned u32x2 __attribute__((ext_vector_type(2)));
typedef unsigned long long u64;
#define DI __device__ __forceinline__

constexpr unsigned BATCH_BYTES = 16 * 71680 + 256;
constexpr unsigned PART_BYTES = 71680;
constexpr size_t WS_FLAG = 131072;
constexpr size_t WS_PART = 262144;

DI unsigned cvtpk(float lo, float hi) { f32x2 v = {lo, hi}; bf16x2 b = __builtin_convertvector(v, bf16x2); return __builtin_bit_cast(unsigned, b); }
DI __amdgpu_buffer_rsrc_t mk(const void* p, unsigned bytes) { return __builtin_amdgcn_make_buffer_rsrc((void*)p, 0, bytes, 0x00020000); }
DI unsigned lane_id() { return __builtin_amdgcn_mbcnt_hi(~0u, __builtin_amdgcn_mbcnt_lo(~0u, 0u)); }

DI void chunk_pos(unsigned ci, unsigned& o0, unsigned& o1) {
  unsigned q0, c0, q1, c1;
  if (ci < 4352u) {
    const unsigned w = ci / 1088u, rem = ci - w * 1088u, pr = rem >> 6, l = rem & 63u, n = l & 15u, g = l >> 4;
    if (pr < 10u) { const unsigned i = pr / 5u, t = pr - i * 5u; q0 = q1 = t * 16u + n; c0 = w * 64u + i * 32u + g * 4u; c1 = c0 + 16u; }
    else if (pr < 15u) { q0 = q1 = (pr - 10u) * 16u + n; c0 = 256u + w * 48u + g * 4u; c1 = c0 + 16u; }
    else { q0 = (pr - 15u) * 32u + n; q1 = q0 + 16u; c0 = c1 = 288u + w * 48u + g * 4u; }
  } else {
    const unsigned e = (ci - 4352u) * 2u, w = e >> 6, l = e & 63u, n = l & 15u, g = l >> 4;
    q0 = 64u + n; q1 = q0 + 1u; c0 = c1 = 288u + w * 48u + g * 4u;
  }
  o0 = q0 * 896u + c0 * 2u; o1 = q1 * 896u + c1 * 2u;
}
#define GQA_SUM16(dst, expr) do { float lo_ = 0.f, hi_ = 0.f; \
    _Pragma("unroll") for (int hh = 0; hh < 16; ++hh) { const unsigned x_ = (expr); lo_ += __uint_as_float(x_ << 16); hi_ += __uint_as_float(x_ & 0xffff0000u); } \
    dst = cvtpk(lo_, hi_); } while (0)
DI void sum_store(const u32x4 (&v)[16], unsigned o0, unsigned o1, const __amdgpu_buffer_rsrc_t ob) {
  unsigned r0, r1, r2, r3;
  GQA_SUM16(r0, v[hh][0]); GQA_SUM16(r1, v[hh][1]); GQA_SUM16(r2, v[hh][2]); GQA_SUM16(r3, v[hh][3]);
  __builtin_amdgcn_raw_buffer_store_b64(u32x2{r0, r1}, ob, o0, 0, 0);
  __builtin_amdgcn_raw_buffer_store_b64(u32x2{r2, r3}, ob, o1, 0, 0);
}

__global__ __launch_bounds__(256, 1) void gqa_main(const char* __restrict__ x, const char* __restrict__ kc, const char* __restrict__ vc,
                                                   const char* __restrict__ wq, const char* __restrict__ wo, char* __restrict__ ws, char* __restrict__ out) {
  extern __shared__ __attribute__((aligned(16))) char smem[];
  const unsigned bid = blockIdx.x, kv = bid & 7u, j = bid >> 3, b = j >> 1, h = kv * 2u + (j & 1u);
  const unsigned wv = __builtin_amdgcn_readfirstlane(threadIdx.x >> 6);
  const unsigned lds0 = (unsigned)(size_t)smem;
  u64 pc; unsigned clen;
  asm volatile("s_getpc_b64 %0\n\tgqa_main_pc:\n\ts_mov_b32 %1, gqa_main_end - gqa_main_pc" : "=s"(pc), "=s"(clen));
  const __amdgpu_buffer_rsrc_t srdC = mk((const void*)pc, clen);
  {
    const unsigned co = threadIdx.x < 12u ? (j * 12u + threadIdx.x) * 128u : 0x7ffffff0u;
    asm volatile("s_mov_b32 m0, %0\n\ts_nop 0\n\tbuffer_load_dword %1, %2, 0 offen lds" :: "s"(lds0 + 76800u), "v"(co), "s"(srdC) : "memory");
    __builtin_amdgcn_sched_barrier(0);
  }
  const unsigned offX = b * 71680u, offK = ((b * 8u + kv) * 512u + wv * 128u) * 512u, offV = (b * 8u + kv) * 262144u + wv * 128u;
  const unsigned offWq = h * 512u + wv * 128u, offWo = h * 229376u;
  const __amdgpu_buffer_rsrc_t srdX = mk(x + offX, 1146880u - offX), srdK = mk(kc + offK, 33554432u - offK), srdV = mk(vc + offV, 33554432u - offV);
  const __amdgpu_buffer_rsrc_t srdWq = mk(wq + offWq, 3670016u - offWq), srdWo = mk(wo + offWo, 3670016u - offWo);
  const __amdgpu_buffer_rsrc_t srdP = mk(ws + WS_PART + (size_t)b * BATCH_BYTES + 128u + h * PART_BYTES, PART_BYTES);
  const __amdgpu_buffer_rsrc_t srdR = mk(ws + WS_PART + (size_t)b * BATCH_BYTES, BATCH_BYTES), srdO = mk(out + (size_t)b * 71680u, 71680u);
  const __amdgpu_buffer_rsrc_t srdF = mk(ws + WS_FLAG + b * 4096u, 4096u);
  unsigned nonce;
  asm volatile(
    "s_lshl_b32 s78, %[wv], 8\n\t"
    "s_add_u32 s78, s78, %[lds0]\n\t"
    "s_add_u32 s78, s78, 75776\n\t"
    "v_mbcnt_lo_u32_b32 v255, -1, 0\n\t"
    "v_mbcnt_hi_u32_b32 v255, -1, v255\n\t"
    "v_lshlrev_b32 v254, 17, v255\n\t"
    "s_lshl_b32 s64, %[bb], 13\n\t"
    "s_mov_b32 m0, s78\n\t"
    "s_nop 0\n\t"
    "buffer_load_dword v254, %[srdWq], s64 offen lds\n\t"
    "s_and_b32 s82, %[wv], 1\n\t"
    "s_mul_i32 s67, s82, 35840\n\t"
    "s_lshr_b32 s82, %[wv], 1\n\t"
    "s_lshl_b32 s82, s82, 7\n\t"
    "s_add_u32 s67, s67, s82\n\t"
    "s_mul_i32 s68, %[wv], 5120\n\t"
    "s_add_u32 s68, s68, %[lds0]\n\t"
    "s_mul_i32 s69, %[wv], 21504\n\t"
    "s_mul_i32 s70, %[wv], 3072\n\t"
    "s_add_u32 s70, s70, %[lds0]\n\t"
    "s_add_u32 s70, s70, 61440\n\t"
    "s_mul_i32 s66, %[wv], 20480\n\t"
    "s_add_u32 s66, s66, %[lds0]\n\t"
    "s_add_u32 s65, s66, 81920\n\t"
    "v_mov_b32 v255, 0\n\t"
    "s_mov_b32 m0, s78\n\t"
    "s_nop 0\n\t"
    "buffer_load_dword v255, %[srdK], 0 offen lds\n\t"
    "buffer_load_dword v255, %[srdV], 0 offen lds\n\t"
    "buffer_load_dword v255, %[srdWo], 0 offen lds\n\t"
    "buffer_load_dword v255, %[srdO], 0 offen lds\n\t"
    "v_mbcnt_lo_u32_b32 v255, -1, 0\n\t"
    "v_mbcnt_hi_u32_b32 v255, -1, v255\n\t"
    "v_and_b32 v254, 1, v255\n\t"
    "v_mul_u32_u24 v254, 1147008, v254\n\t"
    "buffer_load_dword v254, %[srdR], 0 offen lds\n\t"
    "s_lshl_b32 s71, %[wv], 2\n\t"
    "s_add_u32 s71, s71, %[lds0]\n\t"
    "s_mul_i32 s72, %[wv], 17408\n\t"
    "s_lshl_b32 s73, %[wv], 9\n\t"
    "s_add_u32 s73, s73, 69632\n\t"
    "s_lshl_b32 s74, %[hh], 2\n\t"
    "s_lshl_b32 s75, %[hh], 7\n\t"
    "s_add_u32 s75, s75, 128\n\t"
    "s_add_u32 s76, s74, 128\n\t"
    "s_lshl_b32 s81, %[wv], 7\n\t"
    "s_mul_i32 s97, %[wv], 96\n\t"
    "s_add_u32 s97, s97, 512\n\t"
    "s_mov_b32 s79, 0xbdb8aa3b\n\t"
    "s_mov_b32 s80, 0x3db8aa3b\n\t"
    "v_mbcnt_lo_u32_b32 v255, -1, 0\n\t"
    "v_mbcnt_hi_u32_b32 v255, -1, v255\n\t"
    "v_mov_b32 v254, s74\n\t"
    "v_lshl_add_u32 v253, v255, 2, s75\n\t"
    "v_cmp_gt_u32 vcc, 16, v255\n\t"
    "s_nop 1\n\t"
    "v_cndmask_b32 v253, v254, v253, vcc\n\t"
    "buffer_load_dword v255, v253, %[srdF], 0 offen sc1\n\t"
    "v_mbcnt_lo_u32_b32 v253, -1, 0\n\t"
    "v_mbcnt_hi_u32_b32 v253, -1, v253\n\t"
    "v_lshrrev_b32 v252, 3, v253\n\t"
    "v_xor_b32 v251, v253, v252\n\t"
    "v_and_b32 v251, 7, v251\n\t"
    "v_mul_u32_u24 v253, 896, v252\n\t"
    "v_lshl_add_u32 v252, v251, 4, v253\n\t"
    "s_add_u32 m0, s68, 0\n\t"
    "s_add_u32 s64, s67, 0\n\t"
    "buffer_load_dwordx4 v252, %[srdX], s64 offen lds\n\t"
    "s_add_u32 m0, s68, 1024\n\t"
    "s_add_u32 s64, s67, 7168\n\t"
    "buffer_load_dwordx4 v252, %[srdX], s64 offen lds\n\t"
    "s_add_u32 m0, s68, 2048\n\t"
    "s_add_u32 s64, s67, 14336\n\t"
    "buffer_load_dwordx4 v252, %[srdX], s64 offen lds\n\t"
    "s_add_u32 m0, s68, 3072\n\t"
    "s_add_u32 s64, s67, 21504\n\t"
    "buffer_load_dwordx4 v252, %[srdX], s64 offen lds\n\t"
    "s_add_u32 m0, s68, 4096\n\t"
    "s_add_u32 s64, s67, 28672\n\t"
    "buffer_load_dwordx4 v252, %[srdX], s64 offen lds\n\t"
    "s_add_u32 m0, s68, 20480\n\t"
    "s_add_u32 s64, s67, 256\n\t"
    "buffer_load_dwordx4 v252, %[srdX], s64 offen lds\n\t"
    "s_add_u32 m0, s68, 21504\n\t"
    "s_add_u32 s64, s67, 7424\n\t"
    "buffer_load_dwordx4 v252, %[srdX], s64 offen lds\n\t"
    "s_add_u32 m0, s68, 22528\n\t"
    "s_add_u32 s64, s67, 14592\n\t"
    "buffer_load_dwordx4 v252, %[srdX], s64 offen lds\n\t"
    "s_add_u32 m0, s68, 23552\n\t"
    "s_add_u32 s64, s67, 21760\n\t"
    "buffer_load_dwordx4 v252, %[srdX], s64 offen lds\n\t"
    "s_add_u32 m0, s68, 24576\n\t"
    "s_add_u32 s64, s67, 28928\n\t"
    "buffer_load_dwordx4 v252, %[srdX], s64 offen lds\n\t"
    "s_add_u32 m0, s68, 40960\n\t"
    "s_add_u32 s64, s67, 512\n\t"
    "buffer_load_dwordx4 v252, %[srdX], s64 offen lds\n\t"
    "s_add_u32 m0, s68, 41984\n\t"
    "s_add_u32 s64, s67, 7680\n\t"
    "buffer_load_dwordx4 v252, %[srdX], s64 offen lds\n\t"
    "s_add_u32 m0, s68, 43008\n\t"
    "s_add_u32 s64, s67, 14848\n\t"
    "buffer_load_dwordx4 v252, %[srdX], s64 offen lds\n\t"
    "s_add_u32 m0, s68, 44032\n\t"
    "s_add_u32 s64, s67, 22016\n\t"
    "buffer_load_dwordx4 v252, %[srdX], s64 offen lds\n\t"
    "s_add_u32 m0, s68, 45056\n\t"
    "s_add_u32 s64, s67, 29184\n\t"
    "buffer_load_dwordx4 v252, %[srdX], s64 offen lds\n\t"
    "s_add_u32 m0, s70, 0\n\t"
    "s_add_u32 s64, s69, 768\n\t"
    "buffer_load_dwordx4 v252, %[srdX], s64 offen lds\n\t"
    "s_add_u32 m0, s70, 1024\n\t"
    "s_add_u32 s64, s69, 7936\n\t"
    "buffer_load_dwordx4 v252, %[srdX], s64 offen lds\n\t"
    "s_add_u32 m0, s70, 2048\n\t"
    "s_add_u32 s64, s69, 15104\n\t"
    "buffer_load_dwordx4 v252, %[srdX], s64 offen lds\n\t"
    "v_mbcnt_lo_u32_b32 v253, -1, 0\n\t"
    "v_mbcnt_hi_u32_b32 v253, -1, v253\n\t"
    "v_lshrrev_b32 v252, 3, v253\n\t"
    "v_and_b32 v251, 2, v252\n\t"
    "v_xor_b32 v251, v251, v253\n\t"
    "v_and_b32 v251, 7, v251\n\t"
    "v_lshlrev_b32 v253, 13, v252\n\t"
    "v_lshl_add_u32 v252, v251, 4, v253\n\t"
    "v_xor_b32 v253, 64, v252\n\t"
    "v_add_u32 v251, 64512, v253\n\t"
    "v_add_u32 v250, 129024, v252\n\t"
    "v_add_u32 v249, 193536, v253\n\t"
    "s_add_u32 m0, s65, 0\n\t"
    "s_mov_b32 s64, 0\n\t"
    "buffer_load_dwordx4 v252, %[srdWq], s64 offen offset:0 lds\n\t"
    "buffer_load_dwordx4 v251, %[srdWq], s64 offen offset:1024 lds\n\t"
    "buffer_load_dwordx4 v250, %[srdWq], s64 offen offset:2048 lds\n\t"
    "buffer_load_dwordx4 v249, %[srdWq], s64 offen offset:3072 lds\n\t"
    "s_add_u32 m0, s65, 4096\n\t"
    "s_mov_b32 s64, 262144\n\t"
    "buffer_load_dwordx4 v252, %[srdWq], s64 offen offset:0 lds\n\t"
    "buffer_load_dwordx4 v251, %[srdWq], s64 offen offset:1024 lds\n\t"
    "buffer_load_dwordx4 v250, %[srdWq], s64 offen offset:2048 lds\n\t"
    "buffer_load_dwordx4 v249, %[srdWq], s64 offen offset:3072 lds\n\t"
    "s_add_u32 m0, s65, 8192\n\t"
    "s_mov_b32 s64, 524288\n\t"
    "buffer_load_dwordx4 v252, %[srdWq], s64 offen offset:0 lds\n\t"
    "buffer_load_dwordx4 v251, %[srdWq], s64 offen offset:1024 lds\n\t"
    "buffer_load_dwordx4 v250, %[srdWq], s64 offen offset:2048 lds\n\t"
    "buffer_load_dwordx4 v249, %[srdWq], s64 offen offset:3072 lds\n\t"
    "s_add_u32 m0, s65, 12288\n\t"
    "s_mov_b32 s64, 786432\n\t"
    "buffer_load_dwordx4 v252, %[srdWq], s64 offen offset:0 lds\n\t"
    "buffer_load_dwordx4 v251, %[srdWq], s64 offen offset:1024 lds\n\t"
    "buffer_load_dwordx4 v250, %[srdWq], s64 offen offset:2048 lds\n\t"
    "buffer_load_dwordx4 v249, %[srdWq], s64 offen offset:3072 lds\n\t"
    "s_add_u32 m0, s65, 16384\n\t"
    "s_mov_b32 s64, 1048576\n\t"
    "buffer_load_dwordx4 v252, %[srdWq], s64 offen offset:0 lds\n\t"
    "buffer_load_dwordx4 v251, %[srdWq], s64 offen offset:1024 lds\n\t"
    "buffer_load_dwordx4 v250, %[srdWq], s64 offen offset:2048 lds\n\t"
    "buffer_load_dwordx4 v249, %[srdWq], s64 offen offset:3072 lds\n\t"
    "v_mbcnt_lo_u32_b32 v253, -1, 0\n\t"
    "v_mbcnt_hi_u32_b32 v253, -1, v253\n\t"
    "v_and_b32 v248, 7, v253\n\t"
    "v_lshrrev_b32 v247, 4, v253\n\t"
    "v_xor_b32 v246, v248, v247\n\t"
    "v_bfe_u32 v245, v253, 3, 1\n\t"
    "v_lshlrev_b32 v245, 10, v245\n\t"
    "v_lshl_add_u32 v245, v248, 7, v245\n\t"
    "v_lshl_add_u32 v248, v246, 4, v245\n\t"
    "v_add_u32 v246, %[lds0], v248\n\t"
    "v_xor_b32 v245, 64, v248\n\t"
    "v_add_u32 v245, %[lds0], v245\n\t"
    "v_add_u32 v248, 40960, v246\n\t"
    "v_add_u32 v244, 40960, v245\n\t"
    "v_bfe_u32 v243, v253, 2, 2\n\t"
    "v_and_b32 v242, 3, v253\n\t"
    "v_bfe_u32 v241, v253, 3, 2\n\t"
    "v_lshlrev_b32 v253, 10, v247\n\t"
    "v_lshl_add_u32 v253, v243, 7, v253\n\t"
    "v_lshl_add_u32 v253, v242, 3, v253\n\t"
    "v_lshl_add_u32 v247, v241, 5, v253\n\t"
    "v_add_u32 v253, s65, v247\n\t"
    "v_xor_b32 v243, 32, v247\n\t"
    "v_add_u32 v243, s65, v243\n\t"
    "v_xor_b32 v242, 64, v247\n\t"
    "v_add_u32 v242, s65, v242\n\t"
    "v_xor_b32 v241, 96, v247\n\t"
    "v_add_u32 v241, s65, v241\n\t"
    "v_mbcnt_lo_u32_b32 v247, -1, 0\n\t"
    "v_mbcnt_hi_u32_b32 v247, -1, v247\n\t"
    "v_lshrrev_b32 v240, 4, v247\n\t"
    "v_and_b32 v239, 15, v247\n\t"
    "v_xor_b32 v247, v239, v240\n\t"
    "v_lshlrev_b32 v247, 4, v247\n\t"
    "v_lshl_add_u32 v240, v239, 8, v247\n\t"
    "v_add_u32 v247, s65, v240\n\t"
    "v_xor_b32 v239, 64, v240\n\t"
    "v_add_u32 v239, s65, v239\n\t"
    "v_xor_b32 v238, 128, v240\n\t"
    "v_add_u32 v238, s65, v238\n\t"
    "v_xor_b32 v237, 192, v240\n\t"
    "v_add_u32 v237, s65, v237\n\t"
    "s_waitcnt vmcnt(20)\n\t"
    "s_barrier\n\t"
    "ds_read_b128 v[0:3], v246 offset:0\n\t"
    "ds_read_b128 v[4:7], v246 offset:2048\n\t"
    "ds_read_b128 v[8:11], v246 offset:4096\n\t"
    "ds_read_b128 v[12:15], v246 offset:6144\n\t"
    "ds_read_b128 v[16:19], v246 offset:8192\n\t"
    "s_waitcnt vmcnt(16)\n\t"
    "ds_read_b64_tr_b16 v[20:21], v253 offset:0\n\t"
    "ds_read_b64_tr_b16 v[22:23], v253 offset:512\n\t"
    "ds_read_b64_tr_b16 v[24:25], v243 offset:0\n\t"
    "ds_read_b64_tr_b16 v[26:27], v243 offset:512\n\t"
    "ds_read_b64_tr_b16 v[28:29], v242 offset:0\n\t"
    "ds_read_b64_tr_b16 v[30:31], v242 offset:512\n\t"
    "ds_read_b64_tr_b16 v[32:33], v241 offset:0\n\t"
    "ds_read_b64_tr_b16 v[34:35], v241 offset:512\n\t"
    "s_waitcnt lgkmcnt(6)\n\t"
    "v_mfma_f32_16x16x32_bf16 v[36:39], v[20:23], v[0:3], 0\n\t"
    "s_add_u32 m0, s65, 0\n\t"
    "s_mov_b32 s64, 1310720\n\t"
    "s_waitcnt lgkmcnt(0)\n\t"
    "buffer_load_dwordx4 v252, %[srdWq], s64 offen offset:0 lds\n\t"
    "buffer_load_dwordx4 v251, %[srdWq], s64 offen offset:1024 lds\n\t"
    "v_mfma_f32_16x16x32_bf16 v[40:43], v[20:23], v[4:7], 0\n\t"
    "buffer_load_dwordx4 v250, %[srdWq], s64 offen offset:2048 lds\n\t"
    "buffer_load_dwordx4 v249, %[srdWq], s64 offen offset:3072 lds\n\t"
    "ds_read_b128 v[44:47], v245 offset:0\n\t"
    "s_waitcnt vmcnt(16)\n\t"
    "ds_read_b64_tr_b16 v[48:49], v253 offset:4096\n\t"
    "v_mfma_f32_16x16x32_bf16 v[52:55], v[20:23], v[8:11], 0\n\t"
    "ds_read_b64_tr_b16 v[50:51], v253 offset:4608\n\t"
    "ds_read_b64_tr_b16 v[56:57], v243 offset:4096\n\t"
    "ds_read_b64_tr_b16 v[58:59], v243 offset:4608\n\t"
    "ds_read_b64_tr_b16 v[60:61], v242 offset:4096\n\t"
    "v_mfma_f32_16x16x32_bf16 v[64:67], v[20:23], v[12:15], 0\n\t"
    "ds_read_b128 v[68:71], v245 offset:2048\n\t"
    "ds_read_b64_tr_b16 v[62:63], v242 offset:4608\n\t"
    "ds_read_b64_tr_b16 v[72:73], v241 offset:4096\n\t"
    "ds_read_b64_tr_b16 v[74:75], v241 offset:4608\n\t"
    "v_mfma_f32_16x16x32_bf16 v[76:79], v[20:23], v[16:19], 0\n\t"
    "s_waitcnt vmcnt(12)\n\t"
    "ds_read_b64_tr_b16 v[20:21], v253 offset:8192\n\t"
    "ds_read_b64_tr_b16 v[22:23], v253 offset:8704\n\t"
    "ds_read_b64_tr_b16 v[80:81], v243 offset:8192\n\t"
    "ds_read_b64_tr_b16 v[82:83], v243 offset:8704\n\t"
    "v_mfma_f32_16x16x32_bf16 v[84:87], v[24:27], v[0:3], 0\n\t"
    "ds_read_b128 v[88:91], v245 offset:4096\n\t"
    "ds_read_b64_tr_b16 v[92:93], v242 offset:8192\n\t"
    "ds_read_b64_tr_b16 v[94:95], v242 offset:8704\n\t"
    "ds_read_b64_tr_b16 v[96:97], v241 offset:8192\n\t"
    "v_mfma_f32_16x16x32_bf16 v[100:103], v[24:27], v[4:7], 0\n\t"
    "s_add_u32 m0, s65, 4096\n\t"
    "s_mov_b32 s64, 1572864\n\t"
    "s_waitcnt lgkmcnt(8)\n\t"
    "buffer_load_dwordx4 v252, %[srdWq], s64 offen offset:0 lds\n\t"
    "buffer_load_dwordx4 v251, %[srdWq], s64 offen offset:1024 lds\n\t"
    "v_mfma_f32_16x16x32_bf16 v[104:107], v[24:27], v[8:11], 0\n\t"
    "buffer_load_dwordx4 v250, %[srdWq], s64 offen offset:2048 lds\n\t"
    "buffer_load_dwordx4 v249, %[srdWq], s64 offen offset:3072 lds\n\t"
    "ds_read_b128 v[108:111], v245 offset:6144\n\t"
    "ds_read_b64_tr_b16 v[98:99], v241 offset:8704\n\t"
    "v_mfma_f32_16x16x32_bf16 v[112:115], v[24:27], v[12:15], 0\n\t"
    "s_waitcnt vmcnt(12)\n\t"
    "ds_read_b64_tr_b16 v[116:117], v253 offset:12288\n\t"
    "ds_read_b64_tr_b16 v[118:119], v253 offset:12800\n\t"
    "ds_read_b64_tr_b16 v[120:121], v243 offset:12288\n\t"
    "ds_read_b64_tr_b16 v[122:123], v243 offset:12800\n\t"
    "v_mfma_f32_16x16x32_bf16 v[124:127], v[24:27], v[16:19], 0\n\t"
    "ds_read_b128 v[24:27], v245 offset:8192\n\t"
    "ds_read_b64_tr_b16 a[0:1], v242 offset:12288\n\t"
    "ds_read_b64_tr_b16 a[2:3], v242 offset:12800\n\t"
    "ds_read_b64_tr_b16 a[4:5], v241 offset:12288\n\t"
    "v_mfma_f32_16x16x32_bf16 v[128:131], v[28:31], v[0:3], 0\n\t"
    "s_add_u32 m0, s65, 8192\n\t"
    "s_mov_b32 s64, 1835008\n\t"
    "s_waitcnt lgkmcnt(8)\n\t"
    "buffer_load_dwordx4 v252, %[srdWq], s64 offen offset:0 lds\n\t"
    "buffer_load_dwordx4 v251, %[srdWq], s64 offen offset:1024 lds\n\t"
    "v_mfma_f32_16x16x32_bf16 v[132:135], v[28:31], v[4:7], 0\n\t"
    "buffer_load_dwordx4 v250, %[srdWq], s64 offen offset:2048 lds\n\t"
    "buffer_load_dwordx4 v249, %[srdWq], s64 offen offset:3072 lds\n\t"
    "ds_read_b64_tr_b16 a[6:7], v241 offset:12800\n\t"
    "v_mfma_f32_16x16x32_bf16 v[136:139], v[28:31], v[8:11], 0\n\t"
    "v_mfma_f32_16x16x32_bf16 v[140:143], v[28:31], v[12:15], 0\n\t"
    "v_mfma_f32_16x16x32_bf16 v[144:147], v[28:31], v[16:19], 0\n\t"
    "s_add_u32 m0, s65, 12288\n\t"
    "s_mov_b32 s64, 2097152\n\t"
    "s_waitcnt lgkmcnt(0)\n\t"
    "buffer_load_dwordx4 v252, %[srdWq], s64 offen offset:0 lds\n\t"
    "buffer_load_dwordx4 v251, %[srdWq], s64 offen offset:1024 lds\n\t"
    "v_mfma_f32_16x16x32_bf16 v[28:31], v[32:35], v[0:3], 0\n\t"
    "buffer_load_dwordx4 v250, %[srdWq], s64 offen offset:2048 lds\n\t"
    "buffer_load_dwordx4 v249, %[srdWq], s64 offen offset:3072 lds\n\t"
    "v_mfma_f32_16x16x32_bf16 v[0:3], v[32:35], v[4:7], 0\n\t"
    "v_mfma_f32_16x16x32_bf16 v[4:7], v[32:35], v[8:11], 0\n\t"
    "v_mfma_f32_16x16x32_bf16 v[8:11], v[32:35], v[12:15], 0\n\t"
    "v_mfma_f32_16x16x32_bf16 v[12:15], v[32:35], v[16:19], 0\n\t"
    "v_mfma_f32_16x16x32_bf16 v[36:39], v[48:51], v[44:47], v[36:39]\n\t"
    "ds_read_b128 v[16:19], v246 offset:10240\n\t"
    "s_waitcnt vmcnt(16)\n\t"
    "ds_read_b64_tr_b16 a[8:9], v253 offset:16384\n\t"
    "ds_read_b64_tr_b16 a[10:11], v253 offset:16896\n\t"
    "ds_read_b64_tr_b16 a[12:13], v243 offset:16384\n\t"
    "v_mfma_f32_16x16x32_bf16 v[40:43], v[48:51], v[68:71], v[40:43]\n\t"
    "ds_read_b64_tr_b16 a[14:15], v243 offset:16896\n\t"
    "ds_read_b64_tr_b16 a[16:17], v242 offset:16384\n\t"
    "ds_read_b64_tr_b16 a[18:19], v242 offset:16896\n\t"
    "ds_read_b64_tr_b16 a[20:21], v241 offset:16384\n\t"
    "v_mfma_f32_16x16x32_bf16 v[52:55], v[48:51], v[88:91], v[52:55]\n\t"
    "ds_read_b128 v[32:35], v246 offset:12288\n\t"
    "ds_read_b64_tr_b16 a[22:23], v241 offset:16896\n\t"
    "s_waitcnt vmcnt(12)\n\t"
    "ds_read_b64_tr_b16 a[24:25], v253 offset:0\n\t"
    "ds_read_b64_tr_b16 a[26:27], v253 offset:512\n\t"
    "v_mfma_f32_16x16x32_bf16 v[64:67], v[48:51], v[108:111], v[64:67]\n\t"
    "ds_read_b64_tr_b16 a[28:29], v243 offset:0\n\t"
    "ds_read_b64_tr_b16 a[30:31], v243 offset:512\n\t"
    "ds_read_b64_tr_b16 a[32:33], v242 offset:0\n\t"
    "ds_read_b64_tr_b16 a[34:35], v242 offset:512\n\t"
    "v_mfma_f32_16x16x32_bf16 v[76:79], v[48:51], v[24:27], v[76:79]\n\t"
    "ds_read_b128 v[48:51], v246 offset:14336\n\t"
    "ds_read_b64_tr_b16 a[36:37], v241 offset:0\n\t"
    "ds_read_b64_tr_b16 a[38:39], v241 offset:512\n\t"
    "v_mfma_f32_16x16x32_bf16 v[84:87], v[56:59], v[44:47], v[84:87]\n\t"
    "s_add_u32 m0, s65, 16384\n\t"
    "s_mov_b32 s64, 2359296\n\t"
    "s_waitcnt lgkmcnt(9)\n\t"
    "buffer_load_dwordx4 v252, %[srdWq], s64 offen offset:0 lds\n\t"
    "buffer_load_dwordx4 v251, %[srdWq], s64 offen offset:1024 lds\n\t"
    "v_mfma_f32_16x16x32_bf16 v[100:103], v[56:59], v[68:71], v[100:103]\n\t"
    "buffer_load_dwordx4 v250, %[srdWq], s64 offen offset:2048 lds\n\t"
    "buffer_load_dwordx4 v249, %[srdWq], s64 offen offset:3072 lds\n\t"
    "ds_read_b128 v[148:151], v246 offset:16384\n\t"
    "v_mfma_f32_16x16x32_bf16 v[104:107], v[56:59], v[88:91], v[104:107]\n\t"
    "s_add_u32 m0, s65, 0\n\t"
    "s_mov_b32 s64, 2621440\n\t"
    "s_waitcnt lgkmcnt(1)\n\t"
    "buffer_load_dwordx4 v252, %[srdWq], s64 offen offset:0 lds\n\t"
    "buffer_load_dwordx4 v251, %[srdWq], s64 offen offset:1024 lds\n\t"
    "v_mfma_f32_16x16x32_bf16 v[112:115], v[56:59], v[108:111], v[112:115]\n\t"
    "buffer_load_dwordx4 v250, %[srdWq], s64 offen offset:2048 lds\n\t"
    "buffer_load_dwordx4 v249, %[srdWq], s64 offen offset:3072 lds\n\t"
    "ds_read_b128 v[152:155], v246 offset:18432\n\t"
    "s_waitcnt vmcnt(16)\n\t"
    "ds_read_b64_tr_b16 a[40:41], v253 offset:4096\n\t"
    "v_mfma_f32_16x16x32_bf16 v[124:127], v[56:59], v[24:27], v[124:127]\n\t"
    "ds_read_b64_tr_b16 a[42:43], v253 offset:4608\n\t"
    "ds_read_b64_tr_b16 a[44:45], v243 offset:4096\n\t"
    "ds_read_b64_tr_b16 a[46:47], v243 offset:4608\n\t"
    "ds_read_b64_tr_b16 a[48:49], v242 offset:4096\n\t"
    "v_mfma_f32_16x16x32_bf16 v[128:131], v[60:63], v[44:47], v[128:131]\n\t"
    "ds_read_b64_tr_b16 a[50:51], v242 offset:4608\n\t"
    "ds_read_b64_tr_b16 a[52:53], v241 offset:4096\n\t"
    "ds_read_b64_tr_b16 a[54:55], v241 offset:4608\n\t"
    "v_mfma_f32_16x16x32_bf16 v[132:135], v[60:63], v[68:71], v[132:135]\n\t"
    "v_mfma_f32_16x16x32_bf16 v[136:139], v[60:63], v[88:91], v[136:139]\n\t"
    "v_mfma_f32_16x16x32_bf16 v[140:143], v[60:63], v[108:111], v[140:143]\n\t"
    "s_add_u32 m0, s65, 4096\n\t"
    "s_mov_b32 s64, 2883584\n\t"
    "s_waitcnt lgkmcnt(0)\n\t"
    "buffer_load_dwordx4 v252, %[srdWq], s64 offen offset:0 lds\n\t"
    "buffer_load_dwordx4 v251, %[srdWq], s64 offen offset:1024 lds\n\t"
    "v_mfma_f32_16x16x32_bf16 v[144:147], v[60:63], v[24:27], v[144:147]\n\t"
    "buffer_load_dwordx4 v250, %[srdWq], s64 offen offset:2048 lds\n\t"
    "buffer_load_dwordx4 v249, %[srdWq], s64 offen offset:3072 lds\n\t"
    "v_mfma_f32_16x16x32_bf16 v[28:31], v[72:75], v[44:47], v[28:31]\n\t"
    "v_mfma_f32_16x16x32_bf16 v[0:3], v[72:75], v[68:71], v[0:3]\n\t"
    "v_mfma_f32_16x16x32_bf16 v[4:7], v[72:75], v[88:91], v[4:7]\n\t"
    "v_mfma_f32_16x16x32_bf16 v[8:11], v[72:75], v[108:111], v[8:11]\n\t"
    "v_mfma_f32_16x16x32_bf16 v[12:15], v[72:75], v[24:27], v[12:15]\n\t"
    "v_mfma_f32_16x16x32_bf16 v[36:39], v[20:23], v[16:19], v[36:39]\n\t"
    "ds_read_b128 v[24:27], v245 offset:10240\n\t"
    "s_waitcnt vmcnt(16)\n\t"
    "ds_read_b64_tr_b16 a[56:57], v253 offset:8192\n\t"
    "ds_read_b64_tr_b16 a[58:59], v253 offset:8704\n\t"
    "ds_read_b64_tr_b16 a[60:61], v243 offset:8192\n\t"
    "v_mfma_f32_16x16x32_bf16 v[40:43], v[20:23], v[32:35], v[40:43]\n\t"
    "ds_read_b64_tr_b16 a[62:63], v243 offset:8704\n\t"
    "ds_read_b64_tr_b16 a[64:65], v242 offset:8192\n\t"
    "ds_read_b64_tr_b16 a[66:67], v242 offset:8704\n\t"
    "ds_read_b64_tr_b16 a[68:69], v241 offset:8192\n\t"
    "v_mfma_f32_16x16x32_bf16 v[52:55], v[20:23], v[48:51], v[52:55]\n\t"
    "ds_read_b128 v[44:47], v245 offset:12288\n\t"
    "ds_read_b64_tr_b16 a[70:71], v241 offset:8704\n\t"
    "s_waitcnt vmcnt(12)\n\t"
    "ds_read_b64_tr_b16 a[72:73], v253 offset:12288\n\t"
    "ds_read_b64_tr_b16 a[74:75], v253 offset:12800\n\t"
    "v_mfma_f32_16x16x32_bf16 v[64:67], v[20:23], v[148:151], v[64:67]\n\t"
    "ds_read_b64_tr_b16 a[76:77], v243 offset:12288\n\t"
    "ds_read_b64_tr_b16 a[78:79], v243 offset:12800\n\t"
    "ds_read_b64_tr_b16 a[80:81], v242 offset:12288\n\t"
    "ds_read_b64_tr_b16 a[82:83], v242 offset:12800\n\t"
    "v_mfma_f32_16x16x32_bf16 v[76:79], v[20:23], v[152:155], v[76:79]\n\t"
    "ds_read_b128 v[20:23], v245 offset:14336\n\t"
    "ds_read_b64_tr_b16 a[84:85], v241 offset:12288\n\t"
    "ds_read_b64_tr_b16 a[86:87], v241 offset:12800\n\t"
    "v_mfma_f32_16x16x32_bf16 v[84:87], v[80:83], v[16:19], v[84:87]\n\t"
    "s_add_u32 m0, s65, 8192\n\t"
    "s_mov_b32 s64, 3145728\n\t"
    "s_waitcnt lgkmcnt(9)\n\t"
    "buffer_load_dwordx4 v252, %[srdWq], s64 offen offset:0 lds\n\t"
    "buffer_load_dwordx4 v251, %[srdWq], s64 offen offset:1024 lds\n\t"
    "v_mfma_f32_16x16x32_bf16 v[100:103], v[80:83], v[32:35], v[100:103]\n\t"
    "buffer_load_dwordx4 v250, %[srdWq], s64 offen offset:2048 lds\n\t"
    "buffer_load_dwordx4 v249, %[srdWq], s64 offen offset:3072 lds\n\t"
    "ds_read_b128 v[56:59], v245 offset:16384\n\t"
    "v_mfma_f32_16x16x32_bf16 v[104:107], v[80:83], v[48:51], v[104:107]\n\t"
    "s_add_u32 m0, s65, 12288\n\t"
    "s_mov_b32 s64, 3407872\n\t"
    "s_waitcnt lgkmcnt(1)\n\t"
    "buffer_load_dwordx4 v252, %[srdWq], s64 offen offset:0 lds\n\t"
    "buffer_load_dwordx4 v251, %[srdWq], s64 offen offset:1024 lds\n\t"
    "v_mfma_f32_16x16x32_bf16 v[112:115], v[80:83], v[148:151], v[112:115]\n\t"
    "buffer_load_dwordx4 v250, %[srdWq], s64 offen offset:2048 lds\n\t"
    "buffer_load_dwordx4 v249, %[srdWq], s64 offen offset:3072 lds\n\t"
    "ds_read_b128 v[60:63], v245 offset:18432\n\t"
    "s_waitcnt vmcnt(16)\n\t"
    "ds_read_b64_tr_b16 a[88:89], v253 offset:16384\n\t"
    "v_mfma_f32_16x16x32_bf16 v[124:127], v[80:83], v[152:155], v[124:127]\n\t"
    "ds_read_b64_tr_b16 a[90:91], v253 offset:16896\n\t"
    "ds_read_b64_tr_b16 a[92:93], v243 offset:16384\n\t"
    "ds_read_b64_tr_b16 a[94:95], v243 offset:16896\n\t"
    "ds_read_b64_tr_b16 a[96:97], v242 offset:16384\n\t"
    "v_mfma_f32_16x16x32_bf16 v[128:131], v[92:95], v[16:19], v[128:131]\n\t"
    "ds_read_b64_tr_b16 a[98:99], v242 offset:16896\n\t"
    "ds_read_b64_tr_b16 a[100:101], v241 offset:16384\n\t"
    "ds_read_b64_tr_b16 a[102:103], v241 offset:16896\n\t"
    "v_mfma_f32_16x16x32_bf16 v[132:135], v[92:95], v[32:35], v[132:135]\n\t"
    "v_mfma_f32_16x16x32_bf16 v[136:139], v[92:95], v[48:51], v[136:139]\n\t"
    "v_mfma_f32_16x16x32_bf16 v[140:143], v[92:95], v[148:151], v[140:143]\n\t"
    "v_mbcnt_lo_u32_b32 v252, -1, 0\n\t"
    "v_mbcnt_hi_u32_b32 v252, -1, v252\n\t"
    "v_lshrrev_b32 v251, 4, v252\n\t"
    "v_xor_b32 v250, v252, v251\n\t"
    "v_and_b32 v250, 15, v250\n\t"
    "v_lshlrev_b32 v252, 9, v251\n\t"
    "v_lshl_add_u32 v251, v250, 4, v252\n\t"
    "v_xor_b32 v252, 64, v251\n\t"
    "v_add_u32 v252, 1024, v252\n\t"
    "v_xor_b32 v250, 128, v251\n\t"
    "v_add_u32 v250, 2048, v250\n\t"
    "v_xor_b32 v249, 192, v251\n\t"
    "v_add_u32 v249, 3072, v249\n\t"
    "s_add_u32 m0, s65, 16384\n\t"
    "s_mov_b32 s64, 0\n\t"
    "s_waitcnt lgkmcnt(0)\n\t"
    "buffer_load_dwordx4 v251, %[srdK], s64 offen offset:0 nt lds\n\t"
    "v_mfma_f32_16x16x32_bf16 v[144:147], v[92:95], v[152:155], v[144:147]\n\t"
    "buffer_load_dwordx4 v252, %[srdK], s64 offen offset:1024 nt lds\n\t"
    "buffer_load_dwordx4 v250, %[srdK], s64 offen offset:2048 nt lds\n\t"
    "buffer_load_dwordx4 v249, %[srdK], s64 offen offset:3072 nt lds\n\t"
    "v_mfma_f32_16x16x32_bf16 v[28:31], v[96:99], v[16:19], v[28:31]\n\t"
    "v_mfma_f32_16x16x32_bf16 v[0:3], v[96:99], v[32:35], v[0:3]\n\t"
    "v_mfma_f32_16x16x32_bf16 v[4:7], v[96:99], v[48:51], v[4:7]\n\t"
    "v_mfma_f32_16x16x32_bf16 v[8:11], v[96:99], v[148:151], v[8:11]\n\t"
    "v_mfma_f32_16x16x32_bf16 v[12:15], v[96:99], v[152:155], v[12:15]\n\t"
    "v_mfma_f32_16x16x32_bf16 v[36:39], v[116:119], v[24:27], v[36:39]\n\t"
    "ds_read_b128 v[16:19], v246 offset:20480\n\t"
    "s_waitcnt vmcnt(16)\n\t"
    "ds_read_b64_tr_b16 a[104:105], v253 offset:0\n\t"
    "ds_read_b64_tr_b16 a[106:107], v253 offset:512\n\t"
    "ds_read_b64_tr_b16 a[108:109], v243 offset:0\n\t"
    "v_mfma_f32_16x16x32_bf16 v[40:43], v[116:119], v[44:47], v[40:43]\n\t"
    "ds_read_b64_tr_b16 a[110:111], v243 offset:512\n\t"
    "ds_read_b64_tr_b16 a[112:113], v242 offset:0\n\t"
    "ds_read_b64_tr_b16 a[114:115], v242 offset:512\n\t"
    "ds_read_b64_tr_b16 a[116:117], v241 offset:0\n\t"
    "v_mfma_f32_16x16x32_bf16 v[52:55], v[116:119], v[20:23], v[52:55]\n\t"
    "ds_read_b128 v[32:35], v246 offset:22528\n\t"
    "ds_read_b64_tr_b16 a[118:119], v241 offset:512\n\t"
    "s_waitcnt vmcnt(12)\n\t"
    "ds_read_b64_tr_b16 a[120:121], v253 offset:4096\n\t"
    "ds_read_b64_tr_b16 a[122:123], v253 offset:4608\n\t"
    "v_mfma_f32_16x16x32_bf16 v[64:67], v[116:119], v[56:59], v[64:67]\n\t"
    "ds_read_b64_tr_b16 a[124:125], v243 offset:4096\n\t"
    "ds_read_b64_tr_b16 a[126:127], v243 offset:4608\n\t"
    "ds_read_b64_tr_b16 a[128:129], v242 offset:4096\n\t"
    "ds_read_b64_tr_b16 a[130:131], v242 offset:4608\n\t"
    "v_mfma_f32_16x16x32_bf16 v[76:79], v[116:119], v[60:63], v[76:79]\n\t"
    "ds_read_b128 v[48:51], v246 offset:24576\n\t"
    "ds_read_b64_tr_b16 a[132:133], v241 offset:4096\n\t"
    "ds_read_b64_tr_b16 a[134:135], v241 offset:4608\n\t"
    "v_mfma_f32_16x16x32_bf16 v[84:87], v[120:123], v[24:27], v[84:87]\n\t"
    "s_add_u32 m0, s65, 0\n\t"
    "s_mov_b32 s64, 256\n\t"
    "s_waitcnt lgkmcnt(9)\n\t"
    "buffer_load_dwordx4 v251, %[srdK], s64 offen offset:0 nt lds\n\t"
    "buffer_load_dwordx4 v252, %[srdK], s64 offen offset:1024 nt lds\n\t"
    "v_mfma_f32_16x16x32_bf16 v[100:103], v[120:123], v[44:47], v[100:103]\n\t"
    "buffer_load_dwordx4 v250, %[srdK], s64 offen offset:2048 nt lds\n\t"
    "buffer_load_dwordx4 v249, %[srdK], s64 offen offset:3072 nt lds\n\t"
    "ds_read_b128 v[68:71], v246 offset:26624\n\t"
    "v_mfma_f32_16x16x32_bf16 v[104:107], v[120:123], v[20:23], v[104:107]\n\t"
    "s_add_u32 m0, s65, 4096\n\t"
    "s_mov_b32 s64, 8192\n\t"
    "s_waitcnt lgkmcnt(1)\n\t"
    "buffer_load_dwordx4 v251, %[srdK], s64 offen offset:0 nt lds\n\t"
    "v_mfma_f32_16x16x32_bf16 v[112:115], v[120:123], v[56:59], v[112:115]\n\t"
    "buffer_load_dwordx4 v252, %[srdK], s64 offen offset:1024 nt lds\n\t"
    "ds_read_b128 v[72:75], v246 offset:28672\n\t"
    "v_mfma_f32_16x16x32_bf16 v[124:127], v[120:123], v[60:63], v[124:127]\n\t"
    "s_waitcnt vmcnt(14)\n\t"
    "ds_read_b64_tr_b16 a[136:137], v253 offset:8192\n\t"
    "ds_read_b64_tr_b16 a[138:139], v253 offset:8704\n\t"
    "ds_read_b64_tr_b16 a[140:141], v243 offset:8192\n\t"
    "ds_read_b64_tr_b16 a[142:143], v243 offset:8704\n\t"
    "v_mfma_f32_16x16x32_bf16 v[128:131], a[0:3], v[24:27], v[128:131]\n\t"
    "buffer_load_dwordx4 v250, %[srdK], s64 offen offset:2048 nt lds\n\t"
    "ds_read_b64_tr_b16 a[144:145], v242 offset:8192\n\t"
    "ds_read_b64_tr_b16 a[146:147], v242 offset:8704\n\t"
    "ds_read_b64_tr_b16 a[148:149], v241 offset:8192\n\t"
    "v_mfma_f32_16x16x32_bf16 v[132:135], a[0:3], v[44:47], v[132:135]\n\t"
    "ds_read_b64_tr_b16 a[150:151], v241 offset:8704\n\t"
    "v_mfma_f32_16x16x32_bf16 v[136:139], a[0:3], v[20:23], v[136:139]\n\t"
    "buffer_load_dwordx4 v249, %[srdK], s64 offen offset:3072 nt lds\n\t"
    "v_mfma_f32_16x16x32_bf16 v[140:143], a[0:3], v[56:59], v[140:143]\n\t"
    "v_mfma_f32_16x16x32_bf16 v[144:147], a[0:3], v[60:63], v[144:147]\n\t"
    "v_mfma_f32_16x16x32_bf16 v[28:31], a[4:7], v[24:27], v[28:31]\n\t"
    "s_add_u32 m0, s65, 8192\n\t"
    "s_mov_b32 s64, 8448\n\t"
    "s_waitcnt lgkmcnt(0)\n\t"
    "buffer_load_dwordx4 v251, %[srdK], s64 offen offset:0 nt lds\n\t"
    "v_mfma_f32_16x16x32_bf16 v[0:3], a[4:7], v[44:47], v[0:3]\n\t"
    "v_mfma_f32_16x16x32_bf16 v[4:7], a[4:7], v[20:23], v[4:7]\n\t"
    "v_mfma_f32_16x16x32_bf16 v[8:11], a[4:7], v[56:59], v[8:11]\n\t"
    "v_mfma_f32_16x16x32_bf16 v[12:15], a[4:7], v[60:63], v[12:15]\n\t"
    "v_mfma_f32_16x16x32_bf16 v[36:39], a[8:11], v[16:19], v[36:39]\n\t"
    "ds_read_b128 v[20:23], v245 offset:20480\n\t"
    "s_waitcnt vmcnt(13)\n\t"
    "ds_read_b64_tr_b16 a[0:1], v253 offset:12288\n\t"
    "ds_read_b64_tr_b16 a[2:3], v253 offset:12800\n\t"
    "ds_read_b64_tr_b16 a[4:5], v243 offset:12288\n\t"
    "v_mfma_f32_16x16x32_bf16 v[40:43], a[8:11], v[32:35], v[40:43]\n\t"
    "ds_read_b64_tr_b16 a[6:7], v243 offset:12800\n\t"
    "ds_read_b64_tr_b16 a[152:153], v242 offset:12288\n\t"
    "ds_read_b64_tr_b16 a[154:155], v242 offset:12800\n\t"
    "ds_read_b64_tr_b16 a[156:157], v241 offset:12288\n\t"
    "v_mfma_f32_16x16x32_bf16 v[52:55], a[8:11], v[48:51], v[52:55]\n\t"
    "ds_read_b128 v[24:27], v245 offset:22528\n\t"
    "ds_read_b64_tr_b16 a[158:159], v241 offset:12800\n\t"
    "v_mfma_f32_16x16x32_bf16 v[64:67], a[8:11], v[68:71], v[64:67]\n\t"
    "s_mov_b32 %[nonce], 0\n\t"
    "v_cmp_eq_u32 vcc, 17, v255\n\t"
    "v_mfma_f32_16x16x32_bf16 v[76:79], a[8:11], v[72:75], v[76:79]\n\t"
    "ds_read_b128 v[44:47], v245 offset:24576\n\t"
    "v_mfma_f32_16x16x32_bf16 v[84:87], a[12:15], v[16:19], v[84:87]\n\t"
    "s_cmp_eq_u64 vcc, 0\n\t"
    "s_cselect_b32 %[nonce], 17, %[nonce]\n\t"
    "v_mfma_f32_16x16x32_bf16 v[100:103], a[12:15], v[32:35], v[100:103]\n\t"
    "ds_read_b128 v[56:59], v245 offset:26624\n\t"
    "v_cmp_eq_u32 vcc, 16, v255\n\t"
    "v_mfma_f32_16x16x32_bf16 v[104:107], a[12:15], v[48:51], v[104:107]\n\t"
    "s_cmp_eq_u64 vcc, 0\n\t"
    "s_cselect_b32 %[nonce], 16, %[nonce]\n\t"
    "v_mfma_f32_16x16x32_bf16 v[112:115], a[12:15], v[68:71], v[112:115]\n\t"
    "ds_read_b128 v[60:63], v245 offset:28672\n\t"
    "v_cmp_eq_u32 vcc, 15, v255\n\t"
    "v_mfma_f32_16x16x32_bf16 v[124:127], a[12:15], v[72:75], v[124:127]\n\t"
    "s_cmp_eq_u64 vcc, 0\n\t"
    "s_cselect_b32 %[nonce], 15, %[nonce]\n\t"
    "v_mfma_f32_16x16x32_bf16 v[128:131], a[16:19], v[16:19], v[128:131]\n\t"
    "v_cmp_eq_u32 vcc, 14, v255\n\t"
    "v_mfma_f32_16x16x32_bf16 v[132:135], a[16:19], v[32:35], v[132:135]\n\t"
    "s_cmp_eq_u64 vcc, 0\n\t"
    "s_cselect_b32 %[nonce], 14, %[nonce]\n\t"
    "v_mfma_f32_16x16x32_bf16 v[136:139], a[16:19], v[48:51], v[136:139]\n\t"
    "v_cmp_eq_u32 vcc, 13, v255\n\t"
    "v_mfma_f32_16x16x32_bf16 v[140:143], a[16:19], v[68:71], v[140:143]\n\t"
    "v_mfma_f32_16x16x32_bf16 v[144:147], a[16:19], v[72:75], v[144:147]\n\t"
    "v_mfma_f32_16x16x32_bf16 v[28:31], a[20:23], v[16:19], v[28:31]\n\t"
    "v_mfma_f32_16x16x32_bf16 v[0:3], a[20:23], v[32:35], v[0:3]\n\t"
    "v_mfma_f32_16x16x32_bf16 v[4:7], a[20:23], v[48:51], v[4:7]\n\t"
    "v_mfma_f32_16x16x32_bf16 v[8:11], a[20:23], v[68:71], v[8:11]\n\t"
    "v_mfma_f32_16x16x32_bf16 v[12:15], a[20:23], v[72:75], v[12:15]\n\t"
    "s_waitcnt lgkmcnt(12)\n\t"
    "v_mfma_f32_16x16x32_bf16 v[36:39], a[24:27], v[20:23], v[36:39]\n\t"
    "ds_read_b128 v[16:19], v246 offset:30720\n\t"
    "s_waitcnt lgkmcnt(5)\n\t"
    "v_mfma_f32_16x16x32_bf16 v[40:43], a[24:27], v[24:27], v[40:43]\n\t"
    "s_cmp_eq_u64 vcc, 0\n\t"
    "s_cselect_b32 %[nonce], 13, %[nonce]\n\t"
    "s_waitcnt lgkmcnt(3)\n\t"
    "v_mfma_f32_16x16x32_bf16 v[52:55], a[24:27], v[44:47], v[52:55]\n\t"
    "ds_read_b128 v[32:35], v246 offset:32768\n\t"
    "v_cmp_eq_u32 vcc, 12, v255\n\t"
    "s_waitcnt lgkmcnt(3)\n\t"
    "v_mfma_f32_16x16x32_bf16 v[64:67], a[24:27], v[56:59], v[64:67]\n\t"
    "s_cmp_eq_u64 vcc, 0\n\t"
    "s_cselect_b32 %[nonce], 12, %[nonce]\n\t"
    "s_waitcnt lgkmcnt(2)\n\t"
    "v_mfma_f32_16x16x32_bf16 v[76:79], a[24:27], v[60:63], v[76:79]\n\t"
    "ds_read_b128 v[48:51], v246 offset:34816\n\t"
    "v_cmp_eq_u32 vcc, 11, v255\n\t"
    "v_mfma_f32_16x16x32_bf16 v[84:87], a[28:31], v[20:23], v[84:87]\n\t"
    "s_cmp_eq_u64 vcc, 0\n\t"
    "s_cselect_b32 %[nonce], 11, %[nonce]\n\t"
    "v_mfma_f32_16x16x32_bf16 v[100:103], a[28:31], v[24:27], v[100:103]\n\t"
    "ds_read_b128 v[68:71], v246 offset:36864\n\t"
    "v_cmp_eq_u32 vcc, 10, v255\n\t"
    "v_mfma_f32_16x16x32_bf16 v[104:107], a[28:31], v[44:47], v[104:107]\n\t"
    "s_cmp_eq_u64 vcc, 0\n\t"
    "s_cselect_b32 %[nonce], 10, %[nonce]\n\t"
    "v_mfma_f32_16x16x32_bf16 v[112:115], a[28:31], v[56:59], v[112:115]\n\t"
    "ds_read_b128 v[72:75], v246 offset:38912\n\t"
    "v_cmp_eq_u32 vcc, 9, v255\n\t"
    "v_mfma_f32_16x16x32_bf16 v[124:127], a[28:31], v[60:63], v[124:127]\n\t"
    "s_cmp_eq_u64 vcc, 0\n\t"
    "s_cselect_b32 %[nonce], 9, %[nonce]\n\t"
    "v_mfma_f32_16x16x32_bf16 v[128:131], a[32:35], v[20:23], v[128:131]\n\t"
    "v_cmp_eq_u32 vcc, 8, v255\n\t"
    "v_mfma_f32_16x16x32_bf16 v[132:135], a[32:35], v[24:27], v[132:135]\n\t"
    "v_mfma_f32_16x16x32_bf16 v[136:139], a[32:35], v[44:47], v[136:139]\n\t"
    "v_mfma_f32_16x16x32_bf16 v[140:143], a[32:35], v[56:59], v[140:143]\n\t"
    "v_mfma_f32_16x16x32_bf16 v[144:147], a[32:35], v[60:63], v[144:147]\n\t"
    "v_mfma_f32_16x16x32_bf16 v[28:31], a[36:39], v[20:23], v[28:31]\n\t"
    "v_mfma_f32_16x16x32_bf16 v[0:3], a[36:39], v[24:27], v[0:3]\n\t"
    "v_mfma_f32_16x16x32_bf16 v[4:7], a[36:39], v[44:47], v[4:7]\n\t"
    "v_mfma_f32_16x16x32_bf16 v[8:11], a[36:39], v[56:59], v[8:11]\n\t"
    "v_mfma_f32_16x16x32_bf16 v[12:15], a[36:39], v[60:63], v[12:15]\n\t"
    "s_waitcnt lgkmcnt(4)\n\t"
    "v_mfma_f32_16x16x32_bf16 v[36:39], a[40:43], v[16:19], v[36:39]\n\t"
    "buffer_load_dwordx4 v252, %[srdK], s64 offen offset:1024 nt lds\n\t"
    "ds_read_b128 v[20:23], v245 offset:30720\n\t"
    "s_waitcnt lgkmcnt(4)\n\t"
    "v_mfma_f32_16x16x32_bf16 v[40:43], a[40:43], v[32:35], v[40:43]\n\t"
    "s_cmp_eq_u64 vcc, 0\n\t"
    "s_cselect_b32 %[nonce], 8, %[nonce]\n\t"
    "s_waitcnt lgkmcnt(3)\n\t"
    "v_mfma_f32_16x16x32_bf16 v[52:55], a[40:43], v[48:51], v[52:55]\n\t"
    "ds_read_b128 v[24:27], v245 offset:32768\n\t"
    "v_cmp_eq_u32 vcc, 7, v255\n\t"
    "s_waitcnt lgkmcnt(3)\n\t"
    "v_mfma_f32_16x16x32_bf16 v[64:67], a[40:43], v[68:71], v[64:67]\n\t"
    "s_cmp_eq_u64 vcc, 0\n\t"
    "s_cselect_b32 %[nonce], 7, %[nonce]\n\t"
    "s_waitcnt lgkmcnt(2)\n\t"
    "v_mfma_f32_16x16x32_bf16 v[76:79], a[40:43], v[72:75], v[76:79]\n\t"
    "ds_read_b128 v[44:47], v245 offset:34816\n\t"
    "v_cmp_eq_u32 vcc, 6, v255\n\t"
    "v_mfma_f32_16x16x32_bf16 v[84:87], a[44:47], v[16:19], v[84:87]\n\t"
    "s_cmp_eq_u64 vcc, 0\n\t"
    "s_cselect_b32 %[nonce], 6, %[nonce]\n\t"
    "v_mfma_f32_16x16x32_bf16 v[100:103], a[44:47], v[32:35], v[100:103]\n\t"
    "ds_read_b128 v[56:59], v245 offset:36864\n\t"
    "v_cmp_eq_u32 vcc, 5, v255\n\t"
    "v_mfma_f32_16x16x32_bf16 v[104:107], a[44:47], v[48:51], v[104:107]\n\t"
    "buffer_load_dwordx4 v250, %[srdK], s64 offen offset:2048 nt lds\n\t"
    "v_mfma_f32_16x16x32_bf16 v[112:115], a[44:47], v[68:71], v[112:115]\n\t"
    "ds_read_b128 v[60:63], v245 offset:38912\n\t"
    "v_mfma_f32_16x16x32_bf16 v[124:127], a[44:47], v[72:75], v[124:127]\n\t"
    "s_cmp_eq_u64 vcc, 0\n\t"
    "s_cselect_b32 %[nonce], 5, %[nonce]\n\t"
    "v_mfma_f32_16x16x32_bf16 v[128:131], a[48:51], v[16:19], v[128:131]\n\t"
    "v_cmp_eq_u32 vcc, 4, v255\n\t"
    "v_mfma_f32_16x16x32_bf16 v[132:135], a[48:51], v[32:35], v[132:135]\n\t"
    "s_cmp_eq_u64 vcc, 0\n\t"
    "s_cselect_b32 %[nonce], 4, %[nonce]\n\t"
    "v_mfma_f32_16x16x32_bf16 v[136:139], a[48:51], v[48:51], v[136:139]\n\t"
    "v_cmp_eq_u32 vcc, 3, v255\n\t"
    "v_mfma_f32_16x16x32_bf16 v[140:143], a[48:51], v[68:71], v[140:143]\n\t"
    "v_mfma_f32_16x16x32_bf16 v[144:147], a[48:51], v[72:75], v[144:147]\n\t"
    "v_mfma_f32_16x16x32_bf16 v[28:31], a[52:55], v[16:19], v[28:31]\n\t"
    "buffer_load_dwordx4 v249, %[srdK], s64 offen offset:3072 nt lds\n\t"
    "v_mfma_f32_16x16x32_bf16 v[0:3], a[52:55], v[32:35], v[0:3]\n\t"
    "v_mfma_f32_16x16x32_bf16 v[4:7], a[52:55], v[48:51], v[4:7]\n\t"
    "v_mfma_f32_16x16x32_bf16 v[8:11], a[52:55], v[68:71], v[8:11]\n\t"
    "v_mfma_f32_16x16x32_bf16 v[12:15], a[52:55], v[72:75], v[12:15]\n\t"
    "s_waitcnt lgkmcnt(4)\n\t"
    "v_mfma_f32_16x16x32_bf16 v[36:39], a[56:59], v[20:23], v[36:39]\n\t"
    "ds_read_b128 v[16:19], v248 offset:0\n\t"
    "s_waitcnt lgkmcnt(4)\n\t"
    "v_mfma_f32_16x16x32_bf16 v[40:43], a[56:59], v[24:27], v[40:43]\n\t"
    "s_cmp_eq_u64 vcc, 0\n\t"
    "s_cselect_b32 %[nonce], 3, %[nonce]\n\t"
    "s_waitcnt lgkmcnt(3)\n\t"
    "v_mfma_f32_16x16x32_bf16 v[52:55], a[56:59], v[44:47], v[52:55]\n\t"
    "s_add_u32 m0, s65, 12288\n\t"
    "s_mov_b32 s64, 16384\n\t"
    "buffer_load_dwordx4 v251, %[srdK], s64 offen offset:0 nt lds\n\t"
    "s_waitcnt lgkmcnt(2)\n\t"
    "v_mfma_f32_16x16x32_bf16 v[64:67], a[56:59], v[56:59], v[64:67]\n\t"
    "ds_read_b128 v[32:35], v248 offset:2048\n\t"
    "v_cmp_eq_u32 vcc, 2, v255\n\t"
    "s_waitcnt lgkmcnt(2)\n\t"
    "v_mfma_f32_16x16x32_bf16 v[76:79], a[56:59], v[60:63], v[76:79]\n\t"
    "s_cmp_eq_u64 vcc, 0\n\t"
    "s_cselect_b32 %[nonce], 2, %[nonce]\n\t"
    "v_mfma_f32_16x16x32_bf16 v[84:87], a[60:63], v[20:23], v[84:87]\n\t"
    "ds_read_b128 v[48:51], v248 offset:4096\n\t"
    "v_cmp_eq_u32 vcc, 1, v255\n\t"
    "v_mfma_f32_16x16x32_bf16 v[100:103], a[60:63], v[24:27], v[100:103]\n\t"
    "s_cmp_eq_u64 vcc, 0\n\t"
    "s_cselect_b32 %[nonce], 1, %[nonce]\n\t"
    "v_mfma_f32_16x16x32_bf16 v[104:107], a[60:63], v[44:47], v[104:107]\n\t"
    "ds_read_b128 v[68:71], v248 offset:6144\n\t"
    "s_waitcnt vmcnt(13)\n\t"
    "ds_read_b128 a[8:11], v247 offset:16384\n\t"
    "v_mfma_f32_16x16x32_bf16 v[112:115], a[60:63], v[56:59], v[112:115]\n\t"
    "ds_read_b128 a[12:15], v239 offset:16384\n\t"
    "ds_read_b128 a[16:19], v238 offset:16384\n\t"
    "v_mfma_f32_16x16x32_bf16 v[124:127], a[60:63], v[60:63], v[124:127]\n\t"
    "buffer_load_dwordx4 v252, %[srdK], s64 offen offset:1024 nt lds\n\t"
    "ds_read_b128 v[72:75], v248 offset:8192\n\t"
    "v_mfma_f32_16x16x32_bf16 v[128:131], a[64:67], v[20:23], v[128:131]\n\t"
    "ds_read_b128 a[20:23], v237 offset:16384\n\t"
    "v_cmp_eq_u32 vcc, 0, v255\n\t"
    "v_mfma_f32_16x16x32_bf16 v[132:135], a[64:67], v[24:27], v[132:135]\n\t"
    "s_cmp_eq_u64 vcc, 0\n\t"
    "s_cselect_b32 %[nonce], 0, %[nonce]\n\t"
    "v_mfma_f32_16x16x32_bf16 v[136:139], a[64:67], v[44:47], v[136:139]\n\t"
    "v_mfma_f32_16x16x32_bf16 v[140:143], a[64:67], v[56:59], v[140:143]\n\t"
    "v_mfma_f32_16x16x32_bf16 v[144:147], a[64:67], v[60:63], v[144:147]\n\t"
    "v_mfma_f32_16x16x32_bf16 v[28:31], a[68:71], v[20:23], v[28:31]\n\t"
    "v_mfma_f32_16x16x32_bf16 v[0:3], a[68:71], v[24:27], v[0:3]\n\t"
    "v_mfma_f32_16x16x32_bf16 v[4:7], a[68:71], v[44:47], v[4:7]\n\t"
    "buffer_load_dwordx4 v250, %[srdK], s64 offen offset:2048 nt lds\n\t"
    "v_mfma_f32_16x16x32_bf16 v[8:11], a[68:71], v[56:59], v[8:11]\n\t"
    "v_mfma_f32_16x16x32_bf16 v[12:15], a[68:71], v[60:63], v[12:15]\n\t"
    "s_waitcnt lgkmcnt(8)\n\t"
    "v_mfma_f32_16x16x32_bf16 v[36:39], a[72:75], v[16:19], v[36:39]\n\t"
    "ds_read_b128 v[20:23], v244 offset:0\n\t"
    "s_waitcnt lgkmcnt(8)\n\t"
    "v_mfma_f32_16x16x32_bf16 v[40:43], a[72:75], v[32:35], v[40:43]\n\t"
    "s_waitcnt lgkmcnt(7)\n\t"
    "v_mfma_f32_16x16x32_bf16 v[52:55], a[72:75], v[48:51], v[52:55]\n\t"
    "ds_read_b128 v[24:27], v244 offset:2048\n\t"
    "s_waitcnt lgkmcnt(7)\n\t"
    "v_mfma_f32_16x16x32_bf16 v[64:67], a[72:75], v[68:71], v[64:67]\n\t"
    "s_waitcnt lgkmcnt(3)\n\t"
    "v_mfma_f32_16x16x32_bf16 v[76:79], a[72:75], v[72:75], v[76:79]\n\t"
    "buffer_load_dwordx4 v249, %[srdK], s64 offen offset:3072 nt lds\n\t"
    "ds_read_b128 v[44:47], v244 offset:4096\n\t"
    "v_mfma_f32_16x16x32_bf16 v[84:87], a[76:79], v[16:19], v[84:87]\n\t"
    "v_mfma_f32_16x16x32_bf16 v[100:103], a[76:79], v[32:35], v[100:103]\n\t"
    "ds_read_b128 v[56:59], v244 offset:6144\n\t"
    "v_mfma_f32_16x16x32_bf16 v[104:107], a[76:79], v[48:51], v[104:107]\n\t"
    "v_mfma_f32_16x16x32_bf16 v[112:115], a[76:79], v[68:71], v[112:115]\n\t"
    "ds_read_b128 v[60:63], v244 offset:8192\n\t"
    "v_mfma_f32_16x16x32_bf16 v[124:127], a[76:79], v[72:75], v[124:127]\n\t"
    "v_mfma_f32_16x16x32_bf16 v[128:131], a[80:83], v[16:19], v[128:131]\n\t"
    "v_mfma_f32_16x16x32_bf16 v[132:135], a[80:83], v[32:35], v[132:135]\n\t"
    "v_mfma_f32_16x16x32_bf16 v[136:139], a[80:83], v[48:51], v[136:139]\n\t"
    "s_add_u32 m0, s65, 16384\n\t"
    "s_mov_b32 s64, 16640\n\t"
    "s_waitcnt lgkmcnt(5)\n\t"
    "buffer_load_dwordx4 v251, %[srdK], s64 offen offset:0 nt lds\n\t"
    "v_mfma_f32_16x16x32_bf16 v[140:143], a[80:83], v[68:71], v[140:143]\n\t"
    "v_mfma_f32_16x16x32_bf16 v[144:147], a[80:83], v[72:75], v[144:147]\n\t"
    "v_mfma_f32_16x16x32_bf16 v[28:31], a[84:87], v[16:19], v[28:31]\n\t"
    "v_mfma_f32_16x16x32_bf16 v[0:3], a[84:87], v[32:35], v[0:3]\n\t"
    "v_mfma_f32_16x16x32_bf16 v[4:7], a[84:87], v[48:51], v[4:7]\n\t"
    "v_mfma_f32_16x16x32_bf16 v[8:11], a[84:87], v[68:71], v[8:11]\n\t"
    "v_mfma_f32_16x16x32_bf16 v[12:15], a[84:87], v[72:75], v[12:15]\n\t"
    "buffer_load_dwordx4 v252, %[srdK], s64 offen offset:1024 nt lds\n\t"
    "s_cmp_lg_u32 %[wv], 0\n\t"
    "s_cbranch_scc1 L_n0_%=\n\t"
    "v_mov_b32 v255, %[nonce]\n\t"
    "buffer_store_dword v255, v254, %[srdF], 0 offen sc1\n\t"
    "L_n0_%=:\n\t"
    "s_waitcnt lgkmcnt(4)\n\t"
    "v_mfma_f32_16x16x32_bf16 v[36:39], a[88:91], v[20:23], v[36:39]\n\t"
    "ds_read_b128 v[16:19], v248 offset:10240\n\t"
    "s_waitcnt vmcnt(14)\n\t"
    "ds_read_b128 a[24:27], v247 offset:0\n\t"
    "s_waitcnt lgkmcnt(5)\n\t"
    "v_mfma_f32_16x16x32_bf16 v[40:43], a[88:91], v[24:27], v[40:43]\n\t"
    "ds_read_b128 a[28:31], v239 offset:0\n\t"
    "ds_read_b128 a[32:35], v238 offset:0\n\t"
    "s_waitcnt lgkmcnt(6)\n\t"
    "v_mfma_f32_16x16x32_bf16 v[52:55], a[88:91], v[44:47], v[52:55]\n\t"
    "ds_read_b128 v[32:35], v248 offset:12288\n\t"
    "ds_read_b128 a[36:39], v237 offset:0\n\t"
    "s_waitcnt lgkmcnt(7)\n\t"
    "v_mfma_f32_16x16x32_bf16 v[64:67], a[88:91], v[56:59], v[64:67]\n\t"
    "s_waitcnt lgkmcnt(6)\n\t"
    "v_mfma_f32_16x16x32_bf16 v[76:79], a[88:91], v[60:63], v[76:79]\n\t"
    "ds_read_b128 v[48:51], v248 offset:14336\n\t"
    "v_mfma_f32_16x16x32_bf16 v[84:87], a[92:95], v[20:23], v[84:87]\n\t"
    "v_mfma_f32_16x16x32_bf16 v[100:103], a[92:95], v[24:27], v[100:103]\n\t"
    "ds_read_b128 v[68:71], v248 offset:16384\n\t"
    "v_mfma_f32_16x16x32_bf16 v[104:107], a[92:95], v[44:47], v[104:107]\n\t"
    "buffer_load_dwordx4 v250, %[srdK], s64 offen offset:2048 nt lds\n\t"
    "v_mfma_f32_16x16x32_bf16 v[112:115], a[92:95], v[56:59], v[112:115]\n\t"
    "ds_read_b128 v[72:75], v248 offset:18432\n\t"
    "v_mfma_f32_16x16x32_bf16 v[124:127], a[92:95], v[60:63], v[124:127]\n\t"
    "v_mfma_f32_16x16x32_bf16 v[128:131], a[96:99], v[20:23], v[128:131]\n\t"
    "v_mfma_f32_16x16x32_bf16 v[132:135], a[96:99], v[24:27], v[132:135]\n\t"
    "v_mfma_f32_16x16x32_bf16 v[136:139], a[96:99], v[44:47], v[136:139]\n\t"
    "v_mfma_f32_16x16x32_bf16 v[140:143], a[96:99], v[56:59], v[140:143]\n\t"
    "v_mfma_f32_16x16x32_bf16 v[144:147], a[96:99], v[60:63], v[144:147]\n\t"
    "buffer_load_dwordx4 v249, %[srdK], s64 offen offset:3072 nt lds\n\t"
    "v_mfma_f32_16x16x32_bf16 v[28:31], a[100:103], v[20:23], v[28:31]\n\t"
    "v_mfma_f32_16x16x32_bf16 v[0:3], a[100:103], v[24:27], v[0:3]\n\t"
    "v_mfma_f32_16x16x32_bf16 v[4:7], a[100:103], v[44:47], v[4:7]\n\t"
    "v_mfma_f32_16x16x32_bf16 v[8:11], a[100:103], v[56:59], v[8:11]\n\t"
    "v_mfma_f32_16x16x32_bf16 v[12:15], a[100:103], v[60:63], v[12:15]\n\t"
    "s_waitcnt lgkmcnt(8)\n\t"
    "v_mfma_f32_16x16x32_bf16 v[36:39], a[104:107], v[16:19], v[36:39]\n\t"
    "ds_read_b128 v[20:23], v244 offset:10240\n\t"
    "v_mbcnt_lo_u32_b32 v255, -1, 0\n\t"
    "s_waitcnt lgkmcnt(5)\n\t"
    "v_mfma_f32_16x16x32_bf16 v[40:43], a[104:107], v[32:35], v[40:43]\n\t"
    "v_mbcnt_hi_u32_b32 v255, -1, v255\n\t"
    "v_lshl_add_u32 v254, v255, 4, %[lds0]\n\t"
    "s_waitcnt lgkmcnt(3)\n\t"
    "v_mfma_f32_16x16x32_bf16 v[52:55], a[104:107], v[48:51], v[52:55]\n\t"
    "s_add_u32 m0, s65, 0\n\t"
    "s_mov_b32 s64, 24576\n\t"
    "buffer_load_dwordx4 v251, %[srdK], s64 offen offset:0 nt lds\n\t"
    "s_waitcnt lgkmcnt(2)\n\t"
    "v_mfma_f32_16x16x32_bf16 v[64:67], a[104:107], v[68:71], v[64:67]\n\t"
    "ds_read_b128 v[24:27], v244 offset:12288\n\t"
    "v_add_u32 v253, 40960, v254\n\t"
    "s_waitcnt lgkmcnt(2)\n\t"
    "v_mfma_f32_16x16x32_bf16 v[76:79], a[104:107], v[72:75], v[76:79]\n\t"
    "v_lshrrev_b32 v246, 5, v255\n\t"
    "v_lshl_add_u32 v246, v246, 8, s66\n\t"
    "v_mfma_f32_16x16x32_bf16 v[84:87], a[108:111], v[16:19], v[84:87]\n\t"
    "ds_read_b128 v[44:47], v244 offset:14336\n\t"
    "v_and_b32 v245, 15, v255\n\t"
    "v_mfma_f32_16x16x32_bf16 v[100:103], a[108:111], v[32:35], v[100:103]\n\t"
    "v_lshl_add_u32 v246, v245, 4, v246\n\t"
    "v_bfe_u32 v245, v255, 4, 1\n\t"
    "v_mfma_f32_16x16x32_bf16 v[104:107], a[108:111], v[48:51], v[104:107]\n\t"
    "ds_read_b128 v[56:59], v244 offset:16384\n\t"
    "s_waitcnt vmcnt(13)\n\t"
    "ds_read_b128 a[40:43], v247 offset:4096\n\t"
    "v_mfma_f32_16x16x32_bf16 v[112:115], a[108:111], v[68:71], v[112:115]\n\t"
    "ds_read_b128 a[44:47], v239 offset:4096\n\t"
    "ds_read_b128 a[48:51], v238 offset:4096\n\t"
    "v_mfma_f32_16x16x32_bf16 v[124:127], a[108:111], v[72:75], v[124:127]\n\t"
    "buffer_load_dwordx4 v252, %[srdK], s64 offen offset:1024 nt lds\n\t"
    "ds_read_b128 v[60:63], v244 offset:18432\n\t"
    "v_mfma_f32_16x16x32_bf16 v[128:131], a[112:115], v[16:19], v[128:131]\n\t"
    "ds_read_b128 a[52:55], v237 offset:4096\n\t"
    "v_lshl_add_u32 v255, v245, 3, v246\n\t"
    "v_mfma_f32_16x16x32_bf16 v[132:135], a[112:115], v[32:35], v[132:135]\n\t"
    "v_mfma_f32_16x16x32_bf16 v[136:139], a[112:115], v[48:51], v[136:139]\n\t"
    "v_mfma_f32_16x16x32_bf16 v[140:143], a[112:115], v[68:71], v[140:143]\n\t"
    "v_mfma_f32_16x16x32_bf16 v[144:147], a[112:115], v[72:75], v[144:147]\n\t"
    "v_mfma_f32_16x16x32_bf16 v[28:31], a[116:119], v[16:19], v[28:31]\n\t"
    "v_mfma_f32_16x16x32_bf16 v[0:3], a[116:119], v[32:35], v[0:3]\n\t"
    "v_mfma_f32_16x16x32_bf16 v[4:7], a[116:119], v[48:51], v[4:7]\n\t"
    "buffer_load_dwordx4 v250, %[srdK], s64 offen offset:2048 nt lds\n\t"
    "v_mfma_f32_16x16x32_bf16 v[8:11], a[116:119], v[68:71], v[8:11]\n\t"
    "v_mfma_f32_16x16x32_bf16 v[12:15], a[116:119], v[72:75], v[12:15]\n\t"
    "s_waitcnt lgkmcnt(8)\n\t"
    "v_mfma_f32_16x16x32_bf16 v[36:39], a[120:123], v[20:23], v[36:39]\n\t"
    "ds_read_b128 v[16:19], v248 offset:20480\n\t"
    "s_waitcnt lgkmcnt(8)\n\t"
    "v_mfma_f32_16x16x32_bf16 v[40:43], a[120:123], v[24:27], v[40:43]\n\t"
    "s_waitcnt lgkmcnt(7)\n\t"
    "v_mfma_f32_16x16x32_bf16 v[52:55], a[120:123], v[44:47], v[52:55]\n\t"
    "ds_read_b128 v[32:35], v248 offset:22528\n\t"
    "s_waitcnt lgkmcnt(7)\n\t"
    "v_mfma_f32_16x16x32_bf16 v[64:67], a[120:123], v[56:59], v[64:67]\n\t"
    "s_waitcnt lgkmcnt(3)\n\t"
    "v_mfma_f32_16x16x32_bf16 v[76:79], a[120:123], v[60:63], v[76:79]\n\t"
    "buffer_load_dwordx4 v249, %[srdK], s64 offen offset:3072 nt lds\n\t"
    "ds_read_b128 v[48:51], v248 offset:24576\n\t"
    "v_mfma_f32_16x16x32_bf16 v[84:87], a[124:127], v[20:23], v[84:87]\n\t"
    "v_mfma_f32_16x16x32_bf16 v[100:103], a[124:127], v[24:27], v[100:103]\n\t"
    "ds_read_b128 v[68:71], v248 offset:26624\n\t"
    "v_mfma_f32_16x16x32_bf16 v[104:107], a[124:127], v[44:47], v[104:107]\n\t"
    "v_mfma_f32_16x16x32_bf16 v[112:115], a[124:127], v[56:59], v[112:115]\n\t"
    "ds_read_b128 v[72:75], v248 offset:28672\n\t"
    "v_mfma_f32_16x16x32_bf16 v[124:127], a[124:127], v[60:63], v[124:127]\n\t"
    "v_mfma_f32_16x16x32_bf16 v[128:131], a[128:131], v[20:23], v[128:131]\n\t"
    "ds_read_b128 v[80:83], v244 offset:20480\n\t"
    "v_mfma_f32_16x16x32_bf16 v[132:135], a[128:131], v[24:27], v[132:135]\n\t"
    "v_mfma_f32_16x16x32_bf16 v[136:139], a[128:131], v[44:47], v[136:139]\n\t"
    "s_add_u32 m0, s65, 4096\n\t"
    "s_mov_b32 s64, 24832\n\t"
    "s_waitcnt lgkmcnt(6)\n\t"
    "buffer_load_dwordx4 v251, %[srdK], s64 offen offset:0 nt lds\n\t"
    "v_mfma_f32_16x16x32_bf16 v[140:143], a[128:131], v[56:59], v[140:143]\n\t"
    "ds_read_b128 v[88:91], v244 offset:22528\n\t"
    "v_mfma_f32_16x16x32_bf16 v[144:147], a[128:131], v[60:63], v[144:147]\n\t"
    "v_mfma_f32_16x16x32_bf16 v[28:31], a[132:135], v[20:23], v[28:31]\n\t"
    "ds_read_b128 v[20:23], v244 offset:24576\n\t"
    "v_mfma_f32_16x16x32_bf16 v[0:3], a[132:135], v[24:27], v[0:3]\n\t"
    "v_mfma_f32_16x16x32_bf16 v[4:7], a[132:135], v[44:47], v[4:7]\n\t"
    "ds_read_b128 v[24:27], v244 offset:26624\n\t"
    "ds_read_b128 v[44:47], v244 offset:28672\n\t"
    "v_mfma_f32_16x16x32_bf16 v[8:11], a[132:135], v[56:59], v[8:11]\n\t"
    "v_mfma_f32_16x16x32_bf16 v[12:15], a[132:135], v[60:63], v[12:15]\n\t"
    "buffer_load_dwordx4 v252, %[srdK], s64 offen offset:1024 nt lds\n\t"
    "s_waitcnt lgkmcnt(0)\n\t"
    "s_barrier\n\t"
    "v_mfma_f32_16x16x32_bf16 v[36:39], a[136:139], v[16:19], v[36:39]\n\t"
    "buffer_load_dwordx4 v250, %[srdK], s64 offen offset:2048 nt lds\n\t"
    "s_waitcnt vmcnt(15)\n\t"
    "ds_read_b128 a[56:59], v247 offset:8192\n\t"
    "v_mfma_f32_16x16x32_bf16 v[40:43], a[136:139], v[32:35], v[40:43]\n\t"
    "ds_read_b128 a[60:63], v239 offset:8192\n\t"
    "ds_read_b128 a[64:67], v238 offset:8192\n\t"
    "v_mfma_f32_16x16x32_bf16 v[52:55], a[136:139], v[48:51], v[52:55]\n\t"
    "ds_read_b128 a[68:71], v237 offset:8192\n\t"
    "v_mfma_f32_16x16x32_bf16 v[64:67], a[136:139], v[68:71], v[64:67]\n\t"
    "v_mfma_f32_16x16x32_bf16 v[76:79], a[136:139], v[72:75], v[76:79]\n\t"
    "v_mfma_f32_16x16x32_bf16 v[36:39], a[0:3], v[80:83], v[36:39]\n\t"
    "v_mfma_f32_16x16x32_bf16 v[40:43], a[0:3], v[88:91], v[40:43]\n\t"
    "v_mfma_f32_16x16x32_bf16 v[52:55], a[0:3], v[20:23], v[52:55]\n\t"
    "v_mfma_f32_16x16x32_bf16 v[64:67], a[0:3], v[24:27], v[64:67]\n\t"
    "buffer_load_dwordx4 v249, %[srdK], s64 offen offset:3072 nt lds\n\t"
    "v_mfma_f32_16x16x32_bf16 v[76:79], a[0:3], v[44:47], v[76:79]\n\t"
    "s_nop 2\n\t"
    "v_cvt_pk_bf16_f32 v244, v36, v37\n\t"
    "v_cvt_pk_bf16_f32 v245, v38, v39\n\t"
    "v_mfma_f32_16x16x32_bf16 v[84:87], a[140:143], v[16:19], v[84:87]\n\t"
    "ds_write_b64 v255, v[244:245] offset:2048\n\t"
    "s_nop 1\n\t"
    "v_cvt_pk_bf16_f32 v244, v40, v41\n\t"
    "v_mfma_f32_16x16x32_bf16 v[100:103], a[140:143], v[32:35], v[100:103]\n\t"
    "v_cvt_pk_bf16_f32 v245, v42, v43\n\t"
    "ds_write_b64 v255, v[244:245] offset:3072\n\t"
    "v_mfma_f32_16x16x32_bf16 v[104:107], a[140:143], v[48:51], v[104:107]\n\t"
    "s_nop 0\n\t"
    "v_cvt_pk_bf16_f32 v244, v52, v53\n\t"
    "v_cvt_pk_bf16_f32 v245, v54, v55\n\t"
    "v_mfma_f32_16x16x32_bf16 v[112:115], a[140:143], v[68:71], v[112:115]\n\t"
    "ds_write_b64 v255, v[244:245] offset:4096\n\t"
    "s_nop 1\n\t"
    "v_cvt_pk_bf16_f32 v244, v64, v65\n\t"
    "v_mfma_f32_16x16x32_bf16 v[124:127], a[140:143], v[72:75], v[124:127]\n\t"
    "v_cvt_pk_bf16_f32 v245, v66, v67\n\t"
    "ds_write_b64 v255, v[244:245] offset:5120\n\t"
    "v_mfma_f32_16x16x32_bf16 v[84:87], a[4:7], v[80:83], v[84:87]\n\t"
    "s_add_u32 m0, s65, 8192\n\t"
    "s_mov_b32 s64, 32768\n\t"
    "s_waitcnt lgkmcnt(4)\n\t"
    "buffer_load_dwordx4 v251, %[srdK], s64 offen offset:0 nt lds\n\t"
    "v_mfma_f32_16x16x32_bf16 v[100:103], a[4:7], v[88:91], v[100:103]\n\t"
    "v_cvt_pk_bf16_f32 v244, v76, v77\n\t"
    "v_cvt_pk_bf16_f32 v245, v78, v79\n\t"
    "v_mfma_f32_16x16x32_bf16 v[104:107], a[4:7], v[20:23], v[104:107]\n\t"
    "ds_write_b64 v255, v[244:245] offset:6144\n\t"
    "v_mfma_f32_16x16x32_bf16 v[112:115], a[4:7], v[24:27], v[112:115]\n\t"
    "v_mfma_f32_16x16x32_bf16 v[124:127], a[4:7], v[44:47], v[124:127]\n\t"
    "v_cvt_pk_bf16_f32 v244, v84, v85\n\t"
    "v_cvt_pk_bf16_f32 v245, v86, v87\n\t"
    "v_mfma_f32_16x16x32_bf16 v[128:131], a[144:147], v[16:19], v[128:131]\n\t"
    "s_waitcnt vmcnt(13)\n\t"
    "ds_read_b128 a[0:3], v247 offset:12288\n\t"
    "ds_read_b128 a[4:7], v239 offset:12288\n\t"
    "v_mfma_f32_16x16x32_bf16 v[132:135], a[144:147], v[32:35], v[132:135]\n\t"
    "ds_read_b128 a[72:75], v238 offset:12288\n\t"
    "ds_read_b128 a[76:79], v237 offset:12288\n\t"
    "v_mfma_f32_16x16x32_bf16 v[136:139], a[144:147], v[48:51], v[136:139]\n\t"
    "ds_write_b64 v255, v[244:245] offset:2560\n\t"
    "s_nop 1\n\t"
    "v_cvt_pk_bf16_f32 v244, v100, v101\n\t"
    "v_mfma_f32_16x16x32_bf16 v[140:143], a[144:147], v[68:71], v[140:143]\n\t"
    "buffer_load_dwordx4 v252, %[srdK], s64 offen offset:1024 nt lds\n\t"
    "v_cvt_pk_bf16_f32 v245, v102, v103\n\t"
    "v_mfma_f32_16x16x32_bf16 v[144:147], a[144:147], v[72:75], v[144:147]\n\t"
    "ds_write_b64 v255, v[244:245] offset:3584\n\t"
    "s_nop 1\n\t"
    "v_cvt_pk_bf16_f32 v244, v104, v105\n\t"
    "v_mfma_f32_16x16x32_bf16 v[128:131], a[152:155], v[80:83], v[128:131]\n\t"
    "v_cvt_pk_bf16_f32 v245, v106, v107\n\t"
    "ds_write_b64 v255, v[244:245] offset:4608\n\t"
    "v_mfma_f32_16x16x32_bf16 v[132:135], a[152:155], v[88:91], v[132:135]\n\t"
    "s_nop 0\n\t"
    "v_cvt_pk_bf16_f32 v244, v112, v113\n\t"
    "v_cvt_pk_bf16_f32 v245, v114, v115\n\t"
    "v_mfma_f32_16x16x32_bf16 v[136:139], a[152:155], v[20:23], v[136:139]\n\t"
    "ds_write_b64 v255, v[244:245] offset:5632\n\t"
    "s_nop 1\n\t"
    "v_cvt_pk_bf16_f32 v244, v124, v125\n\t"
    "v_mfma_f32_16x16x32_bf16 v[140:143], a[152:155], v[24:27], v[140:143]\n\t"
    "v_cvt_pk_bf16_f32 v245, v126, v127\n\t"
    "ds_write_b64 v255, v[244:245] offset:6656\n\t"
    "v_mfma_f32_16x16x32_bf16 v[144:147], a[152:155], v[44:47], v[144:147]\n\t"
    "s_nop 0\n\t"
    "v_cvt_pk_bf16_f32 v244, v128, v129\n\t"
    "v_cvt_pk_bf16_f32 v245, v130, v131\n\t"
    "v_mfma_f32_16x16x32_bf16 v[28:31], a[148:151], v[16:19], v[28:31]\n\t"
    "buffer_load_dwordx4 v250, %[srdK], s64 offen offset:2048 nt lds\n\t"
    "ds_write_b64 v255, v[244:245] offset:7168\n\t"
    "v_mfma_f32_16x16x32_bf16 v[0:3], a[148:151], v[32:35], v[0:3]\n\t"
    "s_nop 0\n\t"
    "v_cvt_pk_bf16_f32 v244, v132, v133\n\t"
    "v_cvt_pk_bf16_f32 v245, v134, v135\n\t"
    "v_mfma_f32_16x16x32_bf16 v[4:7], a[148:151], v[48:51], v[4:7]\n\t"
    "ds_write_b64 v255, v[244:245] offset:8192\n\t"
    "s_nop 1\n\t"
    "v_cvt_pk_bf16_f32 v244, v136, v137\n\t"
    "v_mfma_f32_16x16x32_bf16 v[8:11], a[148:151], v[68:71], v[8:11]\n\t"
    "v_cvt_pk_bf16_f32 v245, v138, v139\n\t"
    "ds_write_b64 v255, v[244:245] offset:9216\n\t"
    "v_mfma_f32_16x16x32_bf16 v[12:15], a[148:151], v[72:75], v[12:15]\n\t"
    "s_nop 0\n\t"
    "v_cvt_pk_bf16_f32 v244, v140, v141\n\t"
    "v_cvt_pk_bf16_f32 v245, v142, v143\n\t"
    "v_mfma_f32_16x16x32_bf16 v[28:31], a[156:159], v[80:83], v[28:31]\n\t"
    "ds_write_b64 v255, v[244:245] offset:10240\n\t"
    "s_nop 1\n\t"
    "v_cvt_pk_bf16_f32 v244, v144, v145\n\t"
    "v_mfma_f32_16x16x32_bf16 v[0:3], a[156:159], v[88:91], v[0:3]\n\t"
    "v_cvt_pk_bf16_f32 v245, v146, v147\n\t"
    "ds_write_b64 v255, v[244:245] offset:11264\n\t"
    "v_mfma_f32_16x16x32_bf16 v[4:7], a[156:159], v[20:23], v[4:7]\n\t"
    "v_mfma_f32_16x16x32_bf16 v[8:11], a[156:159], v[24:27], v[8:11]\n\t"
    "buffer_load_dwordx4 v249, %[srdK], s64 offen offset:3072 nt lds\n\t"
    "v_mfma_f32_16x16x32_bf16 v[12:15], a[156:159], v[44:47], v[12:15]\n\t"
    "v_cvt_pk_bf16_f32 v244, v28, v29\n\t"
    "v_cvt_pk_bf16_f32 v245, v30, v31\n\t"
    "ds_write_b64 v255, v[244:245] offset:7680\n\t"
    "s_nop 1\n\t"
    "v_cvt_pk_bf16_f32 v244, v0, v1\n\t"
    "v_cvt_pk_bf16_f32 v245, v2, v3\n\t"
    "ds_write_b64 v255, v[244:245] offset:8704\n\t"
    "s_nop 1\n\t"
    "v_cvt_pk_bf16_f32 v244, v4, v5\n\t"
    "v_cvt_pk_bf16_f32 v245, v6, v7\n\t"
    "ds_write_b64 v255, v[244:245] offset:9728\n\t"
    "s_nop 1\n\t"
    "v_cvt_pk_bf16_f32 v244, v8, v9\n\t"
    "v_cvt_pk_bf16_f32 v245, v10, v11\n\t"
    "ds_write_b64 v255, v[244:245] offset:10752\n\t"
    "s_nop 1\n\t"
    "v_cvt_pk_bf16_f32 v244, v12, v13\n\t"
    "v_cvt_pk_bf16_f32 v245, v14, v15\n\t"
    "ds_write_b64 v255, v[244:245] offset:11776\n\t"
    "s_waitcnt lgkmcnt(0)\n\t"
    "s_barrier\n\t"
    "ds_read_b128 a[80:83], v254 offset:2048\n\t"
    "ds_read_b128 a[84:87], v254 offset:3072\n\t"
    "ds_read_b128 a[88:91], v254 offset:4096\n\t"
    "ds_read_b128 a[92:95], v254 offset:5120\n\t"
    "ds_read_b128 a[96:99], v254 offset:6144\n\t"
    "ds_read_b128 a[100:103], v254 offset:7168\n\t"
    "ds_read_b128 a[104:107], v254 offset:8192\n\t"
    "ds_read_b128 a[108:111], v254 offset:9216\n\t"
    "ds_read_b128 a[112:115], v254 offset:10240\n\t"
    "ds_read_b128 a[116:119], v254 offset:11264\n\t"
    "s_waitcnt lgkmcnt(9)\n\t"
    "v_mfma_f32_16x16x32_bf16 v[0:3], a[8:11], a[80:83], 0\n\t"
    "s_add_u32 m0, s65, 12288\n\t"
    "s_mov_b32 s64, 33024\n\t"
    "buffer_load_dwordx4 v251, %[srdK], s64 offen offset:0 nt lds\n\t"
    "s_waitcnt lgkmcnt(8)\n\t"
    "v_mfma_f32_16x16x32_bf16 v[4:7], a[8:11], a[84:87], 0\n\t"
    "buffer_load_dwordx4 v252, %[srdK], s64 offen offset:1024 nt lds\n\t"
    "ds_read_b128 a[120:123], v254 offset:22528\n\t"
    "s_waitcnt lgkmcnt(8)\n\t"
    "v_mfma_f32_16x16x32_bf16 v[8:11], a[8:11], a[88:91], 0\n\t"
    "s_waitcnt vmcnt(14)\n\t"
    "ds_read_b128 v[12:15], v247 offset:16384\n\t"
    "ds_read_b128 v[16:19], v239 offset:16384\n\t"
    "s_waitcnt lgkmcnt(9)\n\t"
    "v_mfma_f32_16x16x32_bf16 v[20:23], a[8:11], a[92:95], 0\n\t"
    "buffer_load_dwordx4 v250, %[srdK], s64 offen offset:2048 nt lds\n\t"
    "ds_read_b128 a[124:127], v254 offset:23552\n\t"
    "s_waitcnt lgkmcnt(9)\n\t"
    "v_mfma_f32_16x16x32_bf16 v[24:27], a[8:11], a[96:99], 0\n\t"
    "ds_read_b128 v[28:31], v238 offset:16384\n\t"
    "ds_read_b128 v[32:35], v237 offset:16384\n\t"
    "s_waitcnt lgkmcnt(10)\n\t"
    "v_mfma_f32_16x16x32_bf16 v[0:3], a[12:15], a[100:103], v[0:3]\n\t"
    "ds_read_b128 a[8:11], v254 offset:24576\n\t"
    "s_waitcnt lgkmcnt(10)\n\t"
    "v_mfma_f32_16x16x32_bf16 v[4:7], a[12:15], a[104:107], v[4:7]\n\t"
    "s_waitcnt lgkmcnt(9)\n\t"
    "v_mfma_f32_16x16x32_bf16 v[8:11], a[12:15], a[108:111], v[8:11]\n\t"
    "ds_read_b128 a[128:131], v254 offset:25600\n\t"
    "s_waitcnt lgkmcnt(9)\n\t"
    "v_mfma_f32_16x16x32_bf16 v[20:23], a[12:15], a[112:115], v[20:23]\n\t"
    "s_waitcnt lgkmcnt(8)\n\t"
    "v_mfma_f32_16x16x32_bf16 v[24:27], a[12:15], a[116:119], v[24:27]\n\t"
    "ds_read_b128 a[12:15], v254 offset:26624\n\t"
    "s_waitcnt lgkmcnt(8)\n\t"
    "v_mfma_f32_16x16x32_bf16 v[0:3], a[16:19], a[120:123], v[0:3]\n\t"
    "ds_read_b128 a[132:135], v254 offset:27648\n\t"
    "s_waitcnt lgkmcnt(6)\n\t"
    "v_mfma_f32_16x16x32_bf16 v[4:7], a[16:19], a[124:127], v[4:7]\n\t"
    "buffer_load_dwordx4 v249, %[srdK], s64 offen offset:3072 nt lds\n\t"
    "ds_read_b128 a[136:139], v254 offset:28672\n\t"
    "s_waitcnt lgkmcnt(4)\n\t"
    "v_mfma_f32_16x16x32_bf16 v[8:11], a[16:19], a[8:11], v[8:11]\n\t"
    "ds_read_b128 a[140:143], v254 offset:29696\n\t"
    "ds_read_b128 a[144:147], v254 offset:30720\n\t"
    "s_waitcnt lgkmcnt(5)\n\t"
    "v_mfma_f32_16x16x32_bf16 v[20:23], a[16:19], a[128:131], v[20:23]\n\t"
    "ds_read_b128 a[148:151], v254 offset:31744\n\t"
    "ds_read_b128 a[152:155], v253 offset:2048\n\t"
    "s_waitcnt lgkmcnt(6)\n\t"
    "v_mfma_f32_16x16x32_bf16 v[24:27], a[16:19], a[12:15], v[24:27]\n\t"
    "ds_read_b128 a[16:19], v253 offset:3072\n\t"
    "ds_read_b128 a[156:159], v253 offset:4096\n\t"
    "s_waitcnt lgkmcnt(7)\n\t"
    "v_mfma_f32_16x16x32_bf16 v[0:3], a[20:23], a[132:135], v[0:3]\n\t"
    "ds_read_b128 a[160:163], v253 offset:5120\n\t"
    "ds_read_b128 a[164:167], v253 offset:6144\n\t"
    "s_waitcnt lgkmcnt(8)\n\t"
    "v_mfma_f32_16x16x32_bf16 v[4:7], a[20:23], a[136:139], v[4:7]\n\t"
    "ds_read_b128 a[168:171], v253 offset:7168\n\t"
    "ds_read_b128 a[172:175], v253 offset:8192\n\t"
    "s_waitcnt lgkmcnt(9)\n\t"
    "v_mfma_f32_16x16x32_bf16 v[8:11], a[20:23], a[140:143], v[8:11]\n\t"
    "ds_read_b128 a[176:179], v253 offset:9216\n\t"
    "ds_read_b128 a[180:183], v253 offset:10240\n\t"
    "s_waitcnt lgkmcnt(10)\n\t"
    "v_mfma_f32_16x16x32_bf16 v[20:23], a[20:23], a[144:147], v[20:23]\n\t"
    "ds_read_b128 a[184:187], v253 offset:11264\n\t"
    "s_waitcnt lgkmcnt(10)\n\t"
    "v_mfma_f32_16x16x32_bf16 v[24:27], a[20:23], a[148:151], v[24:27]\n\t"
    "s_add_u32 m0, s65, 16384\n\t"
    "s_mov_b32 s64, 40960\n\t"
    "buffer_load_dwordx4 v251, %[srdK], s64 offen offset:0 nt lds\n\t"
    "s_waitcnt lgkmcnt(9)\n\t"
    "v_mfma_f32_16x16x32_bf16 v[0:3], a[24:27], a[152:155], v[0:3]\n\t"
    "ds_read_b128 a[20:23], v253 offset:22528\n\t"
    "s_waitcnt lgkmcnt(9)\n\t"
    "v_mfma_f32_16x16x32_bf16 v[4:7], a[24:27], a[16:19], v[4:7]\n\t"
    "s_waitcnt lgkmcnt(8)\n\t"
    "v_mfma_f32_16x16x32_bf16 v[8:11], a[24:27], a[156:159], v[8:11]\n\t"
    "ds_read_b128 a[188:191], v253 offset:23552\n\t"
    "s_waitcnt lgkmcnt(8)\n\t"
    "v_mfma_f32_16x16x32_bf16 v[20:23], a[24:27], a[160:163], v[20:23]\n\t"
    "s_waitcnt lgkmcnt(7)\n\t"
    "v_mfma_f32_16x16x32_bf16 v[24:27], a[24:27], a[164:167], v[24:27]\n\t"
    "ds_read_b128 a[24:27], v253 offset:24576\n\t"
    "s_waitcnt lgkmcnt(7)\n\t"
    "v_mfma_f32_16x16x32_bf16 v[0:3], a[28:31], a[168:171], v[0:3]\n\t"
    "s_waitcnt vmcnt(13)\n\t"
    "ds_read_b128 v[36:39], v247 offset:0\n\t"
    "ds_read_b128 v[40:43], v239 offset:0\n\t"
    "s_waitcnt lgkmcnt(8)\n\t"
    "v_mfma_f32_16x16x32_bf16 v[4:7], a[28:31], a[172:175], v[4:7]\n\t"
    "ds_read_b128 a[192:195], v253 offset:25600\n\t"
    "ds_read_b128 v[44:47], v238 offset:0\n\t"
    "s_waitcnt lgkmcnt(9)\n\t"
    "v_mfma_f32_16x16x32_bf16 v[8:11], a[28:31], a[176:179], v[8:11]\n\t"
    "buffer_load_dwordx4 v252, %[srdK], s64 offen offset:1024 nt lds\n\t"
    "ds_read_b128 v[48:51], v237 offset:0\n\t"
    "s_waitcnt lgkmcnt(9)\n\t"
    "v_mfma_f32_16x16x32_bf16 v[20:23], a[28:31], a[180:183], v[20:23]\n\t"
    "ds_read_b128 a[196:199], v253 offset:26624\n\t"
    "s_waitcnt lgkmcnt(9)\n\t"
    "v_mfma_f32_16x16x32_bf16 v[24:27], a[28:31], a[184:187], v[24:27]\n\t"
    "s_waitcnt lgkmcnt(8)\n\t"
    "v_mfma_f32_16x16x32_bf16 v[0:3], a[32:35], a[20:23], v[0:3]\n\t"
    "ds_read_b128 a[28:31], v253 offset:27648\n\t"
    "s_waitcnt lgkmcnt(8)\n\t"
    "v_mfma_f32_16x16x32_bf16 v[4:7], a[32:35], a[188:191], v[4:7]\n\t"
    "s_waitcnt lgkmcnt(7)\n\t"
    "v_mfma_f32_16x16x32_bf16 v[8:11], a[32:35], a[24:27], v[8:11]\n\t"
    "ds_read_b128 a[200:203], v253 offset:28672\n\t"
    "s_waitcnt lgkmcnt(5)\n\t"
    "v_mfma_f32_16x16x32_bf16 v[20:23], a[32:35], a[192:195], v[20:23]\n\t"
    "s_waitcnt lgkmcnt(2)\n\t"
    "v_mfma_f32_16x16x32_bf16 v[24:27], a[32:35], a[196:199], v[24:27]\n\t"
    "ds_read_b128 a[32:35], v253 offset:29696\n\t"
    "s_waitcnt lgkmcnt(2)\n\t"
    "v_mfma_f32_16x16x32_bf16 v[0:3], a[36:39], a[28:31], v[0:3]\n\t"
    "buffer_load_dwordx4 v250, %[srdK], s64 offen offset:2048 nt lds\n\t"
    "ds_read_b128 a[204:207], v253 offset:30720\n\t"
    "s_waitcnt lgkmcnt(2)\n\t"
    "v_mfma_f32_16x16x32_bf16 v[4:7], a[36:39], a[200:203], v[4:7]\n\t"
    "ds_read_b128 a[208:211], v253 offset:31744\n\t"
    "s_waitcnt lgkmcnt(2)\n\t"
    "v_mfma_f32_16x16x32_bf16 v[8:11], a[36:39], a[32:35], v[8:11]\n\t"
    "s_waitcnt lgkmcnt(1)\n\t"
    "v_mfma_f32_16x16x32_bf16 v[20:23], a[36:39], a[204:207], v[20:23]\n\t"
    "s_waitcnt lgkmcnt(0)\n\t"
    "v_mfma_f32_16x16x32_bf16 v[24:27], a[36:39], a[208:211], v[24:27]\n\t"
    "v_mfma_f32_16x16x32_bf16 v[52:55], a[40:43], a[80:83], 0\n\t"
    "v_mfma_f32_16x16x32_bf16 v[56:59], a[40:43], a[84:87], 0\n\t"
    "v_mfma_f32_16x16x32_bf16 v[60:63], a[40:43], a[88:91], 0\n\t"
    "v_mfma_f32_16x16x32_bf16 v[64:67], a[40:43], a[92:95], 0\n\t"
    "buffer_load_dwordx4 v249, %[srdK], s64 offen offset:3072 nt lds\n\t"
    "v_max3_f32 v255, v0, v1, v2\n\t"
    "v_mfma_f32_16x16x32_bf16 v[68:71], a[40:43], a[96:99], 0\n\t"
    "v_max_f32 v255, v255, v3\n\t"
    "v_max3_f32 v248, v4, v5, v6\n\t"
    "v_mfma_f32_16x16x32_bf16 v[52:55], a[44:47], a[100:103], v[52:55]\n\t"
    "v_max_f32 v248, v248, v7\n\t"
    "v_max3_f32 v246, v8, v9, v10\n\t"
    "v_mfma_f32_16x16x32_bf16 v[56:59], a[44:47], a[104:107], v[56:59]\n\t"
    "v_max_f32 v246, v246, v11\n\t"
    "v_max3_f32 v245, v20, v21, v22\n\t"
    "v_mfma_f32_16x16x32_bf16 v[60:63], a[44:47], a[108:111], v[60:63]\n\t"
    "v_max_f32 v245, v245, v23\n\t"
    "v_max3_f32 v244, v24, v25, v26\n\t"
    "v_mfma_f32_16x16x32_bf16 v[64:67], a[44:47], a[112:115], v[64:67]\n\t"
    "v_max_f32 v244, v244, v27\n\t"
    "v_mfma_f32_16x16x32_bf16 v[68:71], a[44:47], a[116:119], v[68:71]\n\t"
    "v_mfma_f32_16x16x32_bf16 v[52:55], a[48:51], a[120:123], v[52:55]\n\t"
    "v_mfma_f32_16x16x32_bf16 v[56:59], a[48:51], a[124:127], v[56:59]\n\t"
    "v_mfma_f32_16x16x32_bf16 v[60:63], a[48:51], a[8:11], v[60:63]\n\t"
    "s_add_u32 m0, s65, 0\n\t"
    "s_mov_b32 s64, 41216\n\t"
    "buffer_load_dwordx4 v251, %[srdK], s64 offen offset:0 nt lds\n\t"
    "v_mfma_f32_16x16x32_bf16 v[64:67], a[48:51], a[128:131], v[64:67]\n\t"
    "v_mfma_f32_16x16x32_bf16 v[68:71], a[48:51], a[12:15], v[68:71]\n\t"
    "v_mfma_f32_16x16x32_bf16 v[52:55], a[52:55], a[132:135], v[52:55]\n\t"
    "v_mfma_f32_16x16x32_bf16 v[56:59], a[52:55], a[136:139], v[56:59]\n\t"
    "v_mfma_f32_16x16x32_bf16 v[60:63], a[52:55], a[140:143], v[60:63]\n\t"
    "v_mfma_f32_16x16x32_bf16 v[64:67], a[52:55], a[144:147], v[64:67]\n\t"
    "v_mfma_f32_16x16x32_bf16 v[68:71], a[52:55], a[148:151], v[68:71]\n\t"
    "v_mfma_f32_16x16x32_bf16 v[52:55], a[56:59], a[152:155], v[52:55]\n\t"
    "buffer_load_dwordx4 v252, %[srdK], s64 offen offset:1024 nt lds\n\t"
    "s_waitcnt vmcnt(14)\n\t"
    "ds_read_b128 v[72:75], v247 offset:4096\n\t"
    "v_mfma_f32_16x16x32_bf16 v[56:59], a[56:59], a[16:19], v[56:59]\n\t"
    "ds_read_b128 v[76:79], v239 offset:4096\n\t"
    "ds_read_b128 v[80:83], v238 offset:4096\n\t"
    "v_mfma_f32_16x16x32_bf16 v[60:63], a[56:59], a[156:159], v[60:63]\n\t"
    "ds_read_b128 v[84:87], v237 offset:4096\n\t"
    "v_mfma_f32_16x16x32_bf16 v[64:67], a[56:59], a[160:163], v[64:67]\n\t"
    "v_mfma_f32_16x16x32_bf16 v[68:71], a[56:59], a[164:167], v[68:71]\n\t"
    "v_mfma_f32_16x16x32_bf16 v[52:55], a[60:63], a[168:171], v[52:55]\n\t"
    "v_mfma_f32_16x16x32_bf16 v[56:59], a[60:63], a[172:175], v[56:59]\n\t"
    "v_mfma_f32_16x16x32_bf16 v[60:63], a[60:63], a[176:179], v[60:63]\n\t"
    "v_mfma_f32_16x16x32_bf16 v[64:67], a[60:63], a[180:183], v[64:67]\n\t"
    "buffer_load_dwordx4 v250, %[srdK], s64 offen offset:2048 nt lds\n\t"
    "v_mfma_f32_16x16x32_bf16 v[68:71], a[60:63], a[184:187], v[68:71]\n\t"
    "v_mfma_f32_16x16x32_bf16 v[52:55], a[64:67], a[20:23], v[52:55]\n\t"
    "v_mfma_f32_16x16x32_bf16 v[56:59], a[64:67], a[188:191], v[56:59]\n\t"
    "v_mfma_f32_16x16x32_bf16 v[60:63], a[64:67], a[24:27], v[60:63]\n\t"
    "v_mfma_f32_16x16x32_bf16 v[64:67], a[64:67], a[192:195], v[64:67]\n\t"
    "v_mfma_f32_16x16x32_bf16 v[68:71], a[64:67], a[196:199], v[68:71]\n\t"
    "v_mfma_f32_16x16x32_bf16 v[52:55], a[68:71], a[28:31], v[52:55]\n\t"
    "v_mfma_f32_16x16x32_bf16 v[56:59], a[68:71], a[200:203], v[56:59]\n\t"
    "buffer_load_dwordx4 v249, %[srdK], s64 offen offset:3072 nt lds\n\t"
    "v_mfma_f32_16x16x32_bf16 v[60:63], a[68:71], a[32:35], v[60:63]\n\t"
    "v_mfma_f32_16x16x32_bf16 v[64:67], a[68:71], a[204:207], v[64:67]\n\t"
    "v_mfma_f32_16x16x32_bf16 v[68:71], a[68:71], a[208:211], v[68:71]\n\t"
    "v_mfma_f32_16x16x32_bf16 v[88:91], a[0:3], a[80:83], 0\n\t"
    "v_mfma_f32_16x16x32_bf16 v[92:95], a[0:3], a[84:87], 0\n\t"
    "v_mfma_f32_16x16x32_bf16 v[96:99], a[0:3], a[88:91], 0\n\t"
    "v_mfma_f32_16x16x32_bf16 v[100:103], a[0:3], a[92:95], 0\n\t"
    "v_max3_f32 v255, v255, v52, v53\n\t"
    "v_max3_f32 v255, v255, v54, v55\n\t"
    "v_mfma_f32_16x16x32_bf16 v[104:107], a[0:3], a[96:99], 0\n\t"
    "s_add_u32 m0, s65, 4096\n\t"
    "s_mov_b32 s64, 49152\n\t"
    "s_waitcnt lgkmcnt(0)\n\t"
    "buffer_load_dwordx4 v251, %[srdK], s64 offen offset:0 nt lds\n\t"
    "v_mfma_f32_16x16x32_bf16 v[88:91], a[4:7], a[100:103], v[88:91]\n\t"
    "v_max3_f32 v248, v248, v56, v57\n\t"
    "v_max3_f32 v248, v248, v58, v59\n\t"
    "v_mfma_f32_16x16x32_bf16 v[92:95], a[4:7], a[104:107], v[92:95]\n\t"
    "v_max3_f32 v246, v246, v60, v61\n\t"
    "v_max3_f32 v246, v246, v62, v63\n\t"
    "v_mfma_f32_16x16x32_bf16 v[96:99], a[4:7], a[108:111], v[96:99]\n\t"
    "v_max3_f32 v245, v245, v64, v65\n\t"
    "v_max3_f32 v245, v245, v66, v67\n\t"
    "v_mfma_f32_16x16x32_bf16 v[100:103], a[4:7], a[112:115], v[100:103]\n\t"
    "v_max3_f32 v244, v244, v68, v69\n\t"
    "v_max3_f32 v244, v244, v70, v71\n\t"
    "v_mfma_f32_16x16x32_bf16 v[104:107], a[4:7], a[116:119], v[104:107]\n\t"
    "s_waitcnt vmcnt(13)\n\t"
    "ds_read_b128 v[108:111], v247 offset:8192\n\t"
    "ds_read_b128 v[112:115], v239 offset:8192\n\t"
    "v_mfma_f32_16x16x32_bf16 v[88:91], a[72:75], a[120:123], v[88:91]\n\t"
    "ds_read_b128 v[116:119], v238 offset:8192\n\t"
    "ds_read_b128 v[120:123], v237 offset:8192\n\t"
    "v_mfma_f32_16x16x32_bf16 v[92:95], a[72:75], a[124:127], v[92:95]\n\t"
    "v_mfma_f32_16x16x32_bf16 v[96:99], a[72:75], a[8:11], v[96:99]\n\t"
    "buffer_load_dwordx4 v252, %[srdK], s64 offen offset:1024 nt lds\n\t"
    "v_mfma_f32_16x16x32_bf16 v[100:103], a[72:75], a[128:131], v[100:103]\n\t"
    "v_mfma_f32_16x16x32_bf16 v[104:107], a[72:75], a[12:15], v[104:107]\n\t"
    "v_mfma_f32_16x16x32_bf16 v[88:91], a[76:79], a[132:135], v[88:91]\n\t"
    "v_mfma_f32_16x16x32_bf16 v[92:95], a[76:79], a[136:139], v[92:95]\n\t"
    "v_mfma_f32_16x16x32_bf16 v[96:99], a[76:79], a[140:143], v[96:99]\n\t"
    "v_mfma_f32_16x16x32_bf16 v[100:103], a[76:79], a[144:147], v[100:103]\n\t"
    "v_mfma_f32_16x16x32_bf16 v[104:107], a[76:79], a[148:151], v[104:107]\n\t"
    "v_mfma_f32_16x16x32_bf16 v[88:91], v[12:15], a[152:155], v[88:91]\n\t"
    "buffer_load_dwordx4 v250, %[srdK], s64 offen offset:2048 nt lds\n\t"
    "v_mfma_f32_16x16x32_bf16 v[92:95], v[12:15], a[16:19], v[92:95]\n\t"
    "v_mfma_f32_16x16x32_bf16 v[96:99], v[12:15], a[156:159], v[96:99]\n\t"
    "v_mfma_f32_16x16x32_bf16 v[100:103], v[12:15], a[160:163], v[100:103]\n\t"
    "v_mfma_f32_16x16x32_bf16 v[104:107], v[12:15], a[164:167], v[104:107]\n\t"
    "v_mfma_f32_16x16x32_bf16 v[88:91], v[16:19], a[168:171], v[88:91]\n\t"
    "v_mfma_f32_16x16x32_bf16 v[92:95], v[16:19], a[172:175], v[92:95]\n\t"
    "v_mfma_f32_16x16x32_bf16 v[96:99], v[16:19], a[176:179], v[96:99]\n\t"
    "v_mfma_f32_16x16x32_bf16 v[100:103], v[16:19], a[180:183], v[100:103]\n\t"
    "buffer_load_dwordx4 v249, %[srdK], s64 offen offset:3072 nt lds\n\t"
    "v_mfma_f32_16x16x32_bf16 v[104:107], v[16:19], a[184:187], v[104:107]\n\t"
    "v_mfma_f32_16x16x32_bf16 v[88:91], v[28:31], a[20:23], v[88:91]\n\t"
    "v_mfma_f32_16x16x32_bf16 v[92:95], v[28:31], a[188:191], v[92:95]\n\t"
    "v_mfma_f32_16x16x32_bf16 v[96:99], v[28:31], a[24:27], v[96:99]\n\t"
    "v_mfma_f32_16x16x32_bf16 v[100:103], v[28:31], a[192:195], v[100:103]\n\t"
    "v_mfma_f32_16x16x32_bf16 v[104:107], v[28:31], a[196:199], v[104:107]\n\t"
    "v_mfma_f32_16x16x32_bf16 v[88:91], v[32:35], a[28:31], v[88:91]\n\t"
    "v_mfma_f32_16x16x32_bf16 v[92:95], v[32:35], a[200:203], v[92:95]\n\t"
    "s_add_u32 m0, s65, 8192\n\t"
    "s_mov_b32 s64, 49408\n\t"
    "s_waitcnt lgkmcnt(0)\n\t"
    "buffer_load_dwordx4 v251, %[srdK], s64 offen offset:0 nt lds\n\t"
    "v_mfma_f32_16x16x32_bf16 v[96:99], v[32:35], a[32:35], v[96:99]\n\t"
    "v_mfma_f32_16x16x32_bf16 v[100:103], v[32:35], a[204:207], v[100:103]\n\t"
    "v_mfma_f32_16x16x32_bf16 v[104:107], v[32:35], a[208:211], v[104:107]\n\t"
    "v_mfma_f32_16x16x32_bf16 v[12:15], v[36:39], a[80:83], 0\n\t"
    "v_mfma_f32_16x16x32_bf16 v[16:19], v[36:39], a[84:87], 0\n\t"
    "v_mfma_f32_16x16x32_bf16 v[28:31], v[36:39], a[88:91], 0\n\t"
    "s_waitcnt vmcnt(13)\n\t"
    "ds_read_b128 v[32:35], v247 offset:12288\n\t"
    "ds_read_b128 v[124:127], v239 offset:12288\n\t"
    "v_mfma_f32_16x16x32_bf16 v[128:131], v[36:39], a[92:95], 0\n\t"
    "ds_read_b128 v[132:135], v238 offset:12288\n\t"
    "ds_read_b128 v[136:139], v237 offset:12288\n\t"
    "v_mfma_f32_16x16x32_bf16 v[140:143], v[36:39], a[96:99], 0\n\t"
    "buffer_load_dwordx4 v252, %[srdK], s64 offen offset:1024 nt lds\n\t"
    "v_max3_f32 v255, v255, v88, v89\n\t"
    "v_mfma_f32_16x16x32_bf16 v[12:15], v[40:43], a[100:103], v[12:15]\n\t"
    "v_max3_f32 v255, v255, v90, v91\n\t"
    "v_max3_f32 v248, v248, v92, v93\n\t"
    "v_mfma_f32_16x16x32_bf16 v[16:19], v[40:43], a[104:107], v[16:19]\n\t"
    "v_max3_f32 v248, v248, v94, v95\n\t"
    "v_max3_f32 v246, v246, v96, v97\n\t"
    "v_mfma_f32_16x16x32_bf16 v[28:31], v[40:43], a[108:111], v[28:31]\n\t"
    "v_max3_f32 v246, v246, v98, v99\n\t"
    "v_max3_f32 v245, v245, v100, v101\n\t"
    "v_mfma_f32_16x16x32_bf16 v[128:131], v[40:43], a[112:115], v[128:131]\n\t"
    "v_max3_f32 v245, v245, v102, v103\n\t"
    "v_max3_f32 v244, v244, v104, v105\n\t"
    "v_mfma_f32_16x16x32_bf16 v[140:143], v[40:43], a[116:119], v[140:143]\n\t"
    "v_max3_f32 v244, v244, v106, v107\n\t"
    "v_mfma_f32_16x16x32_bf16 v[12:15], v[44:47], a[120:123], v[12:15]\n\t"
    "v_mfma_f32_16x16x32_bf16 v[16:19], v[44:47], a[124:127], v[16:19]\n\t"
    "v_mfma_f32_16x16x32_bf16 v[28:31], v[44:47], a[8:11], v[28:31]\n\t"
    "buffer_load_dwordx4 v250, %[srdK], s64 offen offset:2048 nt lds\n\t"
    "v_mfma_f32_16x16x32_bf16 v[128:131], v[44:47], a[128:131], v[128:131]\n\t"
    "v_mfma_f32_16x16x32_bf16 v[140:143], v[44:47], a[12:15], v[140:143]\n\t"
    "v_mfma_f32_16x16x32_bf16 v[12:15], v[48:51], a[132:135], v[12:15]\n\t"
    "v_mfma_f32_16x16x32_bf16 v[16:19], v[48:51], a[136:139], v[16:19]\n\t"
    "v_mfma_f32_16x16x32_bf16 v[28:31], v[48:51], a[140:143], v[28:31]\n\t"
    "v_mfma_f32_16x16x32_bf16 v[128:131], v[48:51], a[144:147], v[128:131]\n\t"
    "v_mfma_f32_16x16x32_bf16 v[140:143], v[48:51], a[148:151], v[140:143]\n\t"
    "v_mfma_f32_16x16x32_bf16 v[12:15], v[72:75], a[152:155], v[12:15]\n\t"
    "v_mfma_f32_16x16x32_bf16 v[16:19], v[72:75], a[16:19], v[16:19]\n\t"
    "buffer_load_dwordx4 v249, %[srdK], s64 offen offset:3072 nt lds\n\t"
    "v_mfma_f32_16x16x32_bf16 v[28:31], v[72:75], a[156:159], v[28:31]\n\t"
    "v_mfma_f32_16x16x32_bf16 v[128:131], v[72:75], a[160:163], v[128:131]\n\t"
    "v_mfma_f32_16x16x32_bf16 v[140:143], v[72:75], a[164:167], v[140:143]\n\t"
    "v_mfma_f32_16x16x32_bf16 v[12:15], v[76:79], a[168:171], v[12:15]\n\t"
    "v_mfma_f32_16x16x32_bf16 v[16:19], v[76:79], a[172:175], v[16:19]\n\t"
    "v_mfma_f32_16x16x32_bf16 v[28:31], v[76:79], a[176:179], v[28:31]\n\t"
    "v_mfma_f32_16x16x32_bf16 v[128:131], v[76:79], a[180:183], v[128:131]\n\t"
    "v_mfma_f32_16x16x32_bf16 v[140:143], v[76:79], a[184:187], v[140:143]\n\t"
    "s_add_u32 m0, s65, 12288\n\t"
    "s_mov_b32 s64, 57344\n\t"
    "s_waitcnt lgkmcnt(0)\n\t"
    "buffer_load_dwordx4 v251, %[srdK], s64 offen offset:0 nt lds\n\t"
    "v_mfma_f32_16x16x32_bf16 v[12:15], v[80:83], a[20:23], v[12:15]\n\t"
    "v_mfma_f32_16x16x32_bf16 v[16:19], v[80:83], a[188:191], v[16:19]\n\t"
    "v_mfma_f32_16x16x32_bf16 v[28:31], v[80:83], a[24:27], v[28:31]\n\t"
    "v_mfma_f32_16x16x32_bf16 v[128:131], v[80:83], a[192:195], v[128:131]\n\t"
    "v_mfma_f32_16x16x32_bf16 v[140:143], v[80:83], a[196:199], v[140:143]\n\t"
    "s_waitcnt vmcnt(13)\n\t"
    "ds_read_b128 v[36:39], v247 offset:16384\n\t"
    "ds_read_b128 v[40:43], v239 offset:16384\n\t"
    "v_mfma_f32_16x16x32_bf16 v[12:15], v[84:87], a[28:31], v[12:15]\n\t"
    "ds_read_b128 v[44:47], v238 offset:16384\n\t"
    "ds_read_b128 v[48:51], v237 offset:16384\n\t"
    "v_mfma_f32_16x16x32_bf16 v[16:19], v[84:87], a[200:203], v[16:19]\n\t"
    "v_mfma_f32_16x16x32_bf16 v[28:31], v[84:87], a[32:35], v[28:31]\n\t"
    "buffer_load_dwordx4 v252, %[srdK], s64 offen offset:1024 nt lds\n\t"
    "v_mfma_f32_16x16x32_bf16 v[128:131], v[84:87], a[204:207], v[128:131]\n\t"
    "v_mfma_f32_16x16x32_bf16 v[140:143], v[84:87], a[208:211], v[140:143]\n\t"
    "v_mfma_f32_16x16x32_bf16 v[72:75], v[108:111], a[80:83], 0\n\t"
    "v_mfma_f32_16x16x32_bf16 v[76:79], v[108:111], a[84:87], 0\n\t"
    "v_mfma_f32_16x16x32_bf16 v[80:83], v[108:111], a[88:91], 0\n\t"
    "v_mfma_f32_16x16x32_bf16 v[84:87], v[108:111], a[92:95], 0\n\t"
    "v_max3_f32 v255, v255, v12, v13\n\t"
    "v_max3_f32 v255, v255, v14, v15\n\t"
    "v_mfma_f32_16x16x32_bf16 v[144:147], v[108:111], a[96:99], 0\n\t"
    "v_max3_f32 v248, v248, v16, v17\n\t"
    "v_max3_f32 v248, v248, v18, v19\n\t"
    "v_mfma_f32_16x16x32_bf16 v[72:75], v[112:115], a[100:103], v[72:75]\n\t"
    "buffer_load_dwordx4 v250, %[srdK], s64 offen offset:2048 nt lds\n\t"
    "v_max3_f32 v246, v246, v28, v29\n\t"
    "v_mfma_f32_16x16x32_bf16 v[76:79], v[112:115], a[104:107], v[76:79]\n\t"
    "v_max3_f32 v246, v246, v30, v31\n\t"
    "v_max3_f32 v245, v245, v128, v129\n\t"
    "v_mfma_f32_16x16x32_bf16 v[80:83], v[112:115], a[108:111], v[80:83]\n\t"
    "v_max3_f32 v245, v245, v130, v131\n\t"
    "v_max3_f32 v244, v244, v140, v141\n\t"
    "v_mfma_f32_16x16x32_bf16 v[84:87], v[112:115], a[112:115], v[84:87]\n\t"
    "v_max3_f32 v244, v244, v142, v143\n\t"
    "v_mfma_f32_16x16x32_bf16 v[144:147], v[112:115], a[116:119], v[144:147]\n\t"
    "v_mfma_f32_16x16x32_bf16 v[72:75], v[116:119], a[120:123], v[72:75]\n\t"
    "v_mfma_f32_16x16x32_bf16 v[76:79], v[116:119], a[124:127], v[76:79]\n\t"
    "v_mfma_f32_16x16x32_bf16 v[80:83], v[116:119], a[8:11], v[80:83]\n\t"
    "v_mfma_f32_16x16x32_bf16 v[84:87], v[116:119], a[128:131], v[84:87]\n\t"
    "buffer_load_dwordx4 v249, %[srdK], s64 offen offset:3072 nt lds\n\t"
    "v_mfma_f32_16x16x32_bf16 v[144:147], v[116:119], a[12:15], v[144:147]\n\t"
    "v_mfma_f32_16x16x32_bf16 v[72:75], v[120:123], a[132:135], v[72:75]\n\t"
    "v_mfma_f32_16x16x32_bf16 v[76:79], v[120:123], a[136:139], v[76:79]\n\t"
    "v_mfma_f32_16x16x32_bf16 v[80:83], v[120:123], a[140:143], v[80:83]\n\t"
    "v_mfma_f32_16x16x32_bf16 v[84:87], v[120:123], a[144:147], v[84:87]\n\t"
    "v_mfma_f32_16x16x32_bf16 v[144:147], v[120:123], a[148:151], v[144:147]\n\t"
    "v_mfma_f32_16x16x32_bf16 v[72:75], v[32:35], a[152:155], v[72:75]\n\t"
    "v_mfma_f32_16x16x32_bf16 v[76:79], v[32:35], a[16:19], v[76:79]\n\t"
    "s_add_u32 m0, s65, 16384\n\t"
    "s_mov_b32 s64, 57600\n\t"
    "s_waitcnt lgkmcnt(0)\n\t"
    "buffer_load_dwordx4 v251, %[srdK], s64 offen offset:0 nt lds\n\t"
    "v_mfma_f32_16x16x32_bf16 v[80:83], v[32:35], a[156:159], v[80:83]\n\t"
    "v_mfma_f32_16x16x32_bf16 v[84:87], v[32:35], a[160:163], v[84:87]\n\t"
    "v_mfma_f32_16x16x32_bf16 v[144:147], v[32:35], a[164:167], v[144:147]\n\t"
    "v_mfma_f32_16x16x32_bf16 v[72:75], v[124:127], a[168:171], v[72:75]\n\t"
    "v_mfma_f32_16x16x32_bf16 v[76:79], v[124:127], a[172:175], v[76:79]\n\t"
    "s_waitcnt vmcnt(13)\n\t"
    "ds_read_b128 v[32:35], v247 offset:0\n\t"
    "ds_read_b128 v[108:111], v239 offset:0\n\t"
    "v_mfma_f32_16x16x32_bf16 v[80:83], v[124:127], a[176:179], v[80:83]\n\t"
    "ds_read_b128 v[112:115], v238 offset:0\n\t"
    "ds_read_b128 v[116:119], v237 offset:0\n\t"
    "v_mfma_f32_16x16x32_bf16 v[84:87], v[124:127], a[180:183], v[84:87]\n\t"
    "v_mfma_f32_16x16x32_bf16 v[144:147], v[124:127], a[184:187], v[144:147]\n\t"
    "buffer_load_dwordx4 v252, %[srdK], s64 offen offset:1024 nt lds\n\t"
    "v_mfma_f32_16x16x32_bf16 v[72:75], v[132:135], a[20:23], v[72:75]\n\t"
    "v_mfma_f32_16x16x32_bf16 v[76:79], v[132:135], a[188:191], v[76:79]\n\t"
    "v_mfma_f32_16x16x32_bf16 v[80:83], v[132:135], a[24:27], v[80:83]\n\t"
    "v_mfma_f32_16x16x32_bf16 v[84:87], v[132:135], a[192:195], v[84:87]\n\t"
    "v_mfma_f32_16x16x32_bf16 v[144:147], v[132:135], a[196:199], v[144:147]\n\t"
    "v_mfma_f32_16x16x32_bf16 v[72:75], v[136:139], a[28:31], v[72:75]\n\t"
    "v_mfma_f32_16x16x32_bf16 v[76:79], v[136:139], a[200:203], v[76:79]\n\t"
    "v_mfma_f32_16x16x32_bf16 v[80:83], v[136:139], a[32:35], v[80:83]\n\t"
    "buffer_load_dwordx4 v250, %[srdK], s64 offen offset:2048 nt lds\n\t"
    "v_mfma_f32_16x16x32_bf16 v[84:87], v[136:139], a[204:207], v[84:87]\n\t"
    "v_mfma_f32_16x16x32_bf16 v[144:147], v[136:139], a[208:211], v[144:147]\n\t"
    "v_mfma_f32_16x16x32_bf16 v[120:123], v[36:39], a[80:83], 0\n\t"
    "v_mfma_f32_16x16x32_bf16 v[124:127], v[36:39], a[84:87], 0\n\t"
    "v_mfma_f32_16x16x32_bf16 v[132:135], v[36:39], a[88:91], 0\n\t"
    "v_mfma_f32_16x16x32_bf16 v[136:139], v[36:39], a[92:95], 0\n\t"
    "v_max3_f32 v255, v255, v72, v73\n\t"
    "v_max3_f32 v255, v255, v74, v75\n\t"
    "v_mfma_f32_16x16x32_bf16 v[148:151], v[36:39], a[96:99], 0\n\t"
    "v_max3_f32 v248, v248, v76, v77\n\t"
    "v_max3_f32 v248, v248, v78, v79\n\t"
    "v_mfma_f32_16x16x32_bf16 v[120:123], v[40:43], a[100:103], v[120:123]\n\t"
    "buffer_load_dwordx4 v249, %[srdK], s64 offen offset:3072 nt lds\n\t"
    "v_max3_f32 v246, v246, v80, v81\n\t"
    "v_mfma_f32_16x16x32_bf16 v[124:127], v[40:43], a[104:107], v[124:127]\n\t"
    "v_max3_f32 v246, v246, v82, v83\n\t"
    "v_max3_f32 v245, v245, v84, v85\n\t"
    "v_mfma_f32_16x16x32_bf16 v[132:135], v[40:43], a[108:111], v[132:135]\n\t"
    "v_max3_f32 v245, v245, v86, v87\n\t"
    "v_max3_f32 v244, v244, v144, v145\n\t"
    "v_mfma_f32_16x16x32_bf16 v[136:139], v[40:43], a[112:115], v[136:139]\n\t"
    "v_max3_f32 v244, v244, v146, v147\n\t"
    "v_mfma_f32_16x16x32_bf16 v[148:151], v[40:43], a[116:119], v[148:151]\n\t"
    "v_mfma_f32_16x16x32_bf16 v[120:123], v[44:47], a[120:123], v[120:123]\n\t"
    "v_mfma_f32_16x16x32_bf16 v[124:127], v[44:47], a[124:127], v[124:127]\n\t"
    "v_mfma_f32_16x16x32_bf16 v[132:135], v[44:47], a[8:11], v[132:135]\n\t"
    "v_mfma_f32_16x16x32_bf16 v[136:139], v[44:47], a[128:131], v[136:139]\n\t"
    "v_mbcnt_lo_u32_b32 v252, -1, 0\n\t"
    "v_mbcnt_hi_u32_b32 v252, -1, v252\n\t"
    "v_lshlrev_b32 v251, 7, v252\n\t"
    "s_lshl_b32 s64, %[bb], 2\n\t"
    "s_add_u32 s64, s64, %[wv]\n\t"
    "s_mul_i32 s64, s64, 3584\n\t"
    "v_mov_b32 v250, 0x7fffffff\n\t"
    "v_cmp_gt_u32 vcc, 28, v252\n\t"
    "s_nop 1\n\t"
    "v_cndmask_b32 v251, v250, v251, vcc\n\t"
    "s_mov_b32 m0, s78\n\t"
    "s_nop 0\n\t"
    "buffer_load_dword v251, %[srdWo], s64 offen lds\n\t"
    "v_mfma_f32_16x16x32_bf16 v[148:151], v[44:47], a[12:15], v[148:151]\n\t"
    "v_mfma_f32_16x16x32_bf16 v[120:123], v[48:51], a[132:135], v[120:123]\n\t"
    "v_mfma_f32_16x16x32_bf16 v[124:127], v[48:51], a[136:139], v[124:127]\n\t"
    "v_mfma_f32_16x16x32_bf16 v[132:135], v[48:51], a[140:143], v[132:135]\n\t"
    "v_mfma_f32_16x16x32_bf16 v[136:139], v[48:51], a[144:147], v[136:139]\n\t"
    "v_mfma_f32_16x16x32_bf16 v[148:151], v[48:51], a[148:151], v[148:151]\n\t"
    "v_mbcnt_lo_u32_b32 v252, -1, 0\n\t"
    "v_mbcnt_hi_u32_b32 v252, -1, v252\n\t"
    "v_lshrrev_b32 v251, 3, v252\n\t"
    "v_and_b32 v250, 6, v251\n\t"
    "v_xor_b32 v250, v250, v252\n\t"
    "v_and_b32 v250, 7, v250\n\t"
    "v_lshlrev_b32 v249, 9, v251\n\t"
    "v_lshl_add_u32 v251, v250, 4, v249\n\t"
    "v_add_u32 v250, 3072, v251\n\t"
    "v_add_u32 v249, 6144, v251\n\t"
    "v_add_u32 v243, 9216, v251\n\t"
    "v_bfe_u32 v242, v252, 2, 3\n\t"
    "v_bfe_u32 v241, v252, 3, 2\n\t"
    "v_lshrrev_b32 v240, 5, v252\n\t"
    "v_lshlrev_b32 v240, 10, v240\n\t"
    "v_lshl_add_u32 v240, v242, 7, v240\n\t"
    "v_and_b32 v242, 3, v252\n\t"
    "v_lshl_add_u32 v240, v242, 3, v240\n\t"
    "v_lshl_add_u32 v252, v241, 5, v240\n\t"
    "v_add_u32 v242, s65, v252\n\t"
    "v_xor_b32 v241, 32, v252\n\t"
    "v_add_u32 v241, s65, v241\n\t"
    "v_xor_b32 v240, 64, v252\n\t"
    "v_add_u32 v240, s65, v240\n\t"
    "v_xor_b32 v236, 96, v252\n\t"
    "v_add_u32 v236, s65, v236\n\t"
    "s_add_u32 m0, s65, 0\n\t"
    "s_mov_b32 s64, 0\n\t"
    "s_waitcnt lgkmcnt(0)\n\t"
    "buffer_load_dwordx4 v251, %[srdV], s64 offen offset:0 nt lds\n\t"
    "v_mfma_f32_16x16x32_bf16 v[120:123], v[32:35], a[152:155], v[120:123]\n\t"
    "s_waitcnt vmcnt(14)\n\t"
    "ds_read_b128 v[36:39], v247 offset:4096\n\t"
    "ds_read_b128 v[40:43], v239 offset:4096\n\t"
    "v_mfma_f32_16x16x32_bf16 v[124:127], v[32:35], a[16:19], v[124:127]\n\t"
    "buffer_load_dwordx4 v250, %[srdV], s64 offen offset:1024 nt lds\n\t"
    "ds_read_b128 v[44:47], v238 offset:4096\n\t"
    "v_mfma_f32_16x16x32_bf16 v[132:135], v[32:35], a[156:159], v[132:135]\n\t"
    "ds_read_b128 v[48:51], v237 offset:4096\n\t"
    "v_mfma_f32_16x16x32_bf16 v[136:139], v[32:35], a[160:163], v[136:139]\n\t"
    "v_mfma_f32_16x16x32_bf16 v[148:151], v[32:35], a[164:167], v[148:151]\n\t"
    "v_mfma_f32_16x16x32_bf16 v[120:123], v[108:111], a[168:171], v[120:123]\n\t"
    "v_mfma_f32_16x16x32_bf16 v[124:127], v[108:111], a[172:175], v[124:127]\n\t"
    "v_mfma_f32_16x16x32_bf16 v[132:135], v[108:111], a[176:179], v[132:135]\n\t"
    "v_mfma_f32_16x16x32_bf16 v[136:139], v[108:111], a[180:183], v[136:139]\n\t"
    "v_mfma_f32_16x16x32_bf16 v[148:151], v[108:111], a[184:187], v[148:151]\n\t"
    "buffer_load_dwordx4 v249, %[srdV], s64 offen offset:2048 nt lds\n\t"
    "v_mfma_f32_16x16x32_bf16 v[120:123], v[112:115], a[20:23], v[120:123]\n\t"
    "v_mfma_f32_16x16x32_bf16 v[124:127], v[112:115], a[188:191], v[124:127]\n\t"
    "v_mfma_f32_16x16x32_bf16 v[132:135], v[112:115], a[24:27], v[132:135]\n\t"
    "v_mfma_f32_16x16x32_bf16 v[136:139], v[112:115], a[192:195], v[136:139]\n\t"
    "v_mfma_f32_16x16x32_bf16 v[148:151], v[112:115], a[196:199], v[148:151]\n\t"
    "v_mfma_f32_16x16x32_bf16 v[120:123], v[116:119], a[28:31], v[120:123]\n\t"
    "v_mfma_f32_16x16x32_bf16 v[124:127], v[116:119], a[200:203], v[124:127]\n\t"
    "v_mfma_f32_16x16x32_bf16 v[132:135], v[116:119], a[32:35], v[132:135]\n\t"
    "buffer_load_dwordx4 v243, %[srdV], s64 offen offset:3072 nt lds\n\t"
    "v_mfma_f32_16x16x32_bf16 v[136:139], v[116:119], a[204:207], v[136:139]\n\t"
    "v_mfma_f32_16x16x32_bf16 v[148:151], v[116:119], a[208:211], v[148:151]\n\t"
    "s_waitcnt lgkmcnt(3)\n\t"
    "v_mfma_f32_16x16x32_bf16 v[32:35], v[36:39], a[80:83], 0\n\t"
    "v_mfma_f32_16x16x32_bf16 v[108:111], v[36:39], a[84:87], 0\n\t"
    "v_mfma_f32_16x16x32_bf16 v[112:115], v[36:39], a[88:91], 0\n\t"
    "v_mfma_f32_16x16x32_bf16 v[116:119], v[36:39], a[92:95], 0\n\t"
    "s_waitcnt vmcnt(13)\n\t"
    "ds_read_b128 v[152:155], v247 offset:8192\n\t"
    "ds_read_b128 v[156:159], v239 offset:8192\n\t"
    "v_mfma_f32_16x16x32_bf16 v[160:163], v[36:39], a[96:99], 0\n\t"
    "ds_read_b128 v[36:39], v238 offset:8192\n\t"
    "ds_read_b128 v[164:167], v237 offset:8192\n\t"
    "s_waitcnt lgkmcnt(6)\n\t"
    "v_mfma_f32_16x16x32_bf16 v[32:35], v[40:43], a[100:103], v[32:35]\n\t"
    "v_max3_f32 v255, v255, v120, v121\n\t"
    "v_max3_f32 v255, v255, v122, v123\n\t"
    "v_mfma_f32_16x16x32_bf16 v[108:111], v[40:43], a[104:107], v[108:111]\n\t"
    "s_add_u32 m0, s65, 4096\n\t"
    "s_mov_b32 s64, 65536\n\t"
    "s_waitcnt lgkmcnt(4)\n\t"
    "buffer_load_dwordx4 v251, %[srdV], s64 offen offset:0 nt lds\n\t"
    "v_mfma_f32_16x16x32_bf16 v[112:115], v[40:43], a[108:111], v[112:115]\n\t"
    "v_max3_f32 v248, v248, v124, v125\n\t"
    "v_max3_f32 v248, v248, v126, v127\n\t"
    "v_mfma_f32_16x16x32_bf16 v[116:119], v[40:43], a[112:115], v[116:119]\n\t"
    "v_max3_f32 v246, v246, v132, v133\n\t"
    "v_max3_f32 v246, v246, v134, v135\n\t"
    "v_mfma_f32_16x16x32_bf16 v[160:163], v[40:43], a[116:119], v[160:163]\n\t"
    "v_max3_f32 v245, v245, v136, v137\n\t"
    "v_max3_f32 v245, v245, v138, v139\n\t"
    "v_mfma_f32_16x16x32_bf16 v[32:35], v[44:47], a[120:123], v[32:35]\n\t"
    "v_max3_f32 v244, v244, v148, v149\n\t"
    "v_max3_f32 v244, v244, v150, v151\n\t"
    "v_mfma_f32_16x16x32_bf16 v[108:111], v[44:47], a[124:127], v[108:111]\n\t"
    "v_mfma_f32_16x16x32_bf16 v[112:115], v[44:47], a[8:11], v[112:115]\n\t"
    "v_mfma_f32_16x16x32_bf16 v[116:119], v[44:47], a[128:131], v[116:119]\n\t"
    "buffer_load_dwordx4 v250, %[srdV], s64 offen offset:1024 nt lds\n\t"
    "v_mfma_f32_16x16x32_bf16 v[160:163], v[44:47], a[12:15], v[160:163]\n\t"
    "v_mfma_f32_16x16x32_bf16 v[32:35], v[48:51], a[132:135], v[32:35]\n\t"
    "v_mfma_f32_16x16x32_bf16 v[108:111], v[48:51], a[136:139], v[108:111]\n\t"
    "v_mfma_f32_16x16x32_bf16 v[112:115], v[48:51], a[140:143], v[112:115]\n\t"
    "v_mfma_f32_16x16x32_bf16 v[116:119], v[48:51], a[144:147], v[116:119]\n\t"
    "v_mfma_f32_16x16x32_bf16 v[160:163], v[48:51], a[148:151], v[160:163]\n\t"
    "s_waitcnt lgkmcnt(3)\n\t"
    "v_mfma_f32_16x16x32_bf16 v[32:35], v[152:155], a[152:155], v[32:35]\n\t"
    "v_mbcnt_lo_u32_b32 v252, -1, 0\n\t"
    "v_mbcnt_hi_u32_b32 v252, -1, v252\n\t"
    "v_mfma_f32_16x16x32_bf16 v[108:111], v[152:155], a[16:19], v[108:111]\n\t"
    "v_lshl_add_u32 v235, v252, 4, s66\n\t"
    "v_lshrrev_b32 v234, 4, v252\n\t"
    "v_mfma_f32_16x16x32_bf16 v[112:115], v[152:155], a[156:159], v[112:115]\n\t"
    "buffer_load_dwordx4 v249, %[srdV], s64 offen offset:2048 nt lds\n\t"
    "v_mul_u32_u24 v234, 20480, v234\n\t"
    "v_mfma_f32_16x16x32_bf16 v[116:119], v[152:155], a[160:163], v[116:119]\n\t"
    "v_and_b32 v233, 15, v252\n\t"
    "v_lshl_add_u32 v234, v233, 4, v234\n\t"
    "v_mfma_f32_16x16x32_bf16 v[160:163], v[152:155], a[164:167], v[160:163]\n\t"
    "v_add_u32 v252, s71, v234\n\t"
    "v_lshl_add_u32 v234, v233, 4, s66\n\t"
    "s_waitcnt lgkmcnt(2)\n\t"
    "v_mfma_f32_16x16x32_bf16 v[32:35], v[156:159], a[168:171], v[32:35]\n\t"
    "v_mfma_f32_16x16x32_bf16 v[108:111], v[156:159], a[172:175], v[108:111]\n\t"
    "v_mfma_f32_16x16x32_bf16 v[112:115], v[156:159], a[176:179], v[112:115]\n\t"
    "v_mfma_f32_16x16x32_bf16 v[116:119], v[156:159], a[180:183], v[116:119]\n\t"
    "v_mfma_f32_16x16x32_bf16 v[160:163], v[156:159], a[184:187], v[160:163]\n\t"
    "s_waitcnt lgkmcnt(1)\n\t"
    "v_mfma_f32_16x16x32_bf16 v[32:35], v[36:39], a[20:23], v[32:35]\n\t"
    "buffer_load_dwordx4 v243, %[srdV], s64 offen offset:3072 nt lds\n\t"
    "v_mfma_f32_16x16x32_bf16 v[108:111], v[36:39], a[188:191], v[108:111]\n\t"
    "v_mfma_f32_16x16x32_bf16 v[112:115], v[36:39], a[24:27], v[112:115]\n\t"
    "v_mfma_f32_16x16x32_bf16 v[116:119], v[36:39], a[192:195], v[116:119]\n\t"
    "v_mfma_f32_16x16x32_bf16 v[160:163], v[36:39], a[196:199], v[160:163]\n\t"
    "s_waitcnt vmcnt(13)\n\t"
    "ds_read_b128 v[36:39], v247 offset:12288\n\t"
    "ds_read_b128 v[40:43], v239 offset:12288\n\t"
    "s_waitcnt lgkmcnt(2)\n\t"
    "v_mfma_f32_16x16x32_bf16 v[32:35], v[164:167], a[28:31], v[32:35]\n\t"
    "ds_read_b128 v[44:47], v238 offset:12288\n\t"
    "ds_read_b128 v[48:51], v237 offset:12288\n\t"
    "v_mfma_f32_16x16x32_bf16 v[108:111], v[164:167], a[200:203], v[108:111]\n\t"
    "v_mfma_f32_16x16x32_bf16 v[112:115], v[164:167], a[32:35], v[112:115]\n\t"
    "v_mfma_f32_16x16x32_bf16 v[116:119], v[164:167], a[204:207], v[116:119]\n\t"
    "s_add_u32 m0, s65, 8192\n\t"
    "s_mov_b32 s64, 131072\n\t"
    "buffer_load_dwordx4 v251, %[srdV], s64 offen offset:0 nt lds\n\t"
    "v_mfma_f32_16x16x32_bf16 v[160:163], v[164:167], a[208:211], v[160:163]\n\t"
    "s_waitcnt lgkmcnt(3)\n\t"
    "v_mfma_f32_16x16x32_bf16 v[152:155], v[36:39], a[80:83], 0\n\t"
    "v_mfma_f32_16x16x32_bf16 v[156:159], v[36:39], a[84:87], 0\n\t"
    "v_mfma_f32_16x16x32_bf16 v[164:167], v[36:39], a[88:91], 0\n\t"
    "v_mfma_f32_16x16x32_bf16 v[168:171], v[36:39], a[92:95], 0\n\t"
    "v_max3_f32 v255, v255, v32, v33\n\t"
    "v_max3_f32 v255, v255, v34, v35\n\t"
    "v_mfma_f32_16x16x32_bf16 v[172:175], v[36:39], a[96:99], 0\n\t"
    "v_max3_f32 v248, v248, v108, v109\n\t"
    "v_max3_f32 v248, v248, v110, v111\n\t"
    "s_waitcnt lgkmcnt(2)\n\t"
    "v_mfma_f32_16x16x32_bf16 v[152:155], v[40:43], a[100:103], v[152:155]\n\t"
    "v_max3_f32 v246, v246, v112, v113\n\t"
    "v_max3_f32 v246, v246, v114, v115\n\t"
    "v_mfma_f32_16x16x32_bf16 v[156:159], v[40:43], a[104:107], v[156:159]\n\t"
    "buffer_load_dwordx4 v250, %[srdV], s64 offen offset:1024 nt lds\n\t"
    "v_max3_f32 v245, v245, v116, v117\n\t"
    "v_mfma_f32_16x16x32_bf16 v[164:167], v[40:43], a[108:111], v[164:167]\n\t"
    "v_max3_f32 v245, v245, v118, v119\n\t"
    "v_max3_f32 v244, v244, v160, v161\n\t"
    "v_mfma_f32_16x16x32_bf16 v[168:171], v[40:43], a[112:115], v[168:171]\n\t"
    "v_max3_f32 v244, v244, v162, v163\n\t"
    "v_mfma_f32_16x16x32_bf16 v[172:175], v[40:43], a[116:119], v[172:175]\n\t"
    "s_waitcnt lgkmcnt(1)\n\t"
    "v_mfma_f32_16x16x32_bf16 v[152:155], v[44:47], a[120:123], v[152:155]\n\t"
    "v_mfma_f32_16x16x32_bf16 v[156:159], v[44:47], a[124:127], v[156:159]\n\t"
    "v_mfma_f32_16x16x32_bf16 v[164:167], v[44:47], a[8:11], v[164:167]\n\t"
    "v_mfma_f32_16x16x32_bf16 v[168:171], v[44:47], a[128:131], v[168:171]\n\t"
    "v_mfma_f32_16x16x32_bf16 v[172:175], v[44:47], a[12:15], v[172:175]\n\t"
    "buffer_load_dwordx4 v249, %[srdV], s64 offen offset:2048 nt lds\n\t"
    "s_waitcnt vmcnt(12)\n\t"
    "ds_read_b128 v[36:39], v247 offset:16384\n\t"
    "s_waitcnt lgkmcnt(1)\n\t"
    "v_mfma_f32_16x16x32_bf16 v[152:155], v[48:51], a[132:135], v[152:155]\n\t"
    "buffer_load_dwordx4 v243, %[srdV], s64 offen offset:3072 nt lds\n\t"
    "ds_read_b128 v[40:43], v239 offset:16384\n\t"
    "ds_read_b128 v[44:47], v238 offset:16384\n\t"
    "ds_read_b128 v[176:179], v237 offset:16384\n\t"
    "v_mfma_f32_16x16x32_bf16 v[156:159], v[48:51], a[136:139], v[156:159]\n\t"
    "s_add_u32 m0, s65, 12288\n\t"
    "s_mov_b32 s64, 196608\n\t"
    "buffer_load_dwordx4 v251, %[srdV], s64 offen offset:0 nt lds\n\t"
    "v_mfma_f32_16x16x32_bf16 v[164:167], v[48:51], a[140:143], v[164:167]\n\t"
    "v_mfma_f32_16x16x32_bf16 v[168:171], v[48:51], a[144:147], v[168:171]\n\t"
    "v_mfma_f32_16x16x32_bf16 v[172:175], v[48:51], a[148:151], v[172:175]\n\t"
    "s_waitcnt lgkmcnt(3)\n\t"
    "v_mfma_f32_16x16x32_bf16 v[152:155], v[36:39], a[152:155], v[152:155]\n\t"
    "v_mfma_f32_16x16x32_bf16 v[156:159], v[36:39], a[16:19], v[156:159]\n\t"
    "v_mfma_f32_16x16x32_bf16 v[164:167], v[36:39], a[156:159], v[164:167]\n\t"
    "v_mfma_f32_16x16x32_bf16 v[168:171], v[36:39], a[160:163], v[168:171]\n\t"
    "v_mfma_f32_16x16x32_bf16 v[172:175], v[36:39], a[164:167], v[172:175]\n\t"
    "buffer_load_dwordx4 v250, %[srdV], s64 offen offset:1024 nt lds\n\t"
    "s_waitcnt lgkmcnt(2)\n\t"
    "v_mfma_f32_16x16x32_bf16 v[152:155], v[40:43], a[168:171], v[152:155]\n\t"
    "v_mfma_f32_16x16x32_bf16 v[156:159], v[40:43], a[172:175], v[156:159]\n\t"
    "v_mfma_f32_16x16x32_bf16 v[164:167], v[40:43], a[176:179], v[164:167]\n\t"
    "v_mfma_f32_16x16x32_bf16 v[168:171], v[40:43], a[180:183], v[168:171]\n\t"
    "v_mfma_f32_16x16x32_bf16 v[172:175], v[40:43], a[184:187], v[172:175]\n\t"
    "s_waitcnt lgkmcnt(1)\n\t"
    "v_mfma_f32_16x16x32_bf16 v[152:155], v[44:47], a[20:23], v[152:155]\n\t"
    "v_mfma_f32_16x16x32_bf16 v[156:159], v[44:47], a[188:191], v[156:159]\n\t"
    "v_mfma_f32_16x16x32_bf16 v[164:167], v[44:47], a[24:27], v[164:167]\n\t"
    "buffer_load_dwordx4 v249, %[srdV], s64 offen offset:2048 nt lds\n\t"
    "v_mfma_f32_16x16x32_bf16 v[168:171], v[44:47], a[192:195], v[168:171]\n\t"
    "v_mfma_f32_16x16x32_bf16 v[172:175], v[44:47], a[196:199], v[172:175]\n\t"
    "s_waitcnt lgkmcnt(0)\n\t"
    "v_mfma_f32_16x16x32_bf16 v[152:155], v[176:179], a[28:31], v[152:155]\n\t"
    "v_mfma_f32_16x16x32_bf16 v[156:159], v[176:179], a[200:203], v[156:159]\n\t"
    "v_mfma_f32_16x16x32_bf16 v[164:167], v[176:179], a[32:35], v[164:167]\n\t"
    "v_mfma_f32_16x16x32_bf16 v[168:171], v[176:179], a[204:207], v[168:171]\n\t"
    "v_mfma_f32_16x16x32_bf16 v[172:175], v[176:179], a[208:211], v[172:175]\n\t"
    "s_nop 3\n\t"
    "v_max3_f32 v255, v255, v152, v153\n\t"
    "buffer_load_dwordx4 v243, %[srdV], s64 offen offset:3072 nt lds\n\t"
    "v_max3_f32 v255, v255, v154, v155\n\t"
    "v_max3_f32 v248, v248, v156, v157\n\t"
    "v_max3_f32 v248, v248, v158, v159\n\t"
    "v_max3_f32 v246, v246, v164, v165\n\t"
    "v_max3_f32 v246, v246, v166, v167\n\t"
    "v_max3_f32 v245, v245, v168, v169\n\t"
    "v_max3_f32 v245, v245, v170, v171\n\t"
    "v_max3_f32 v244, v244, v172, v173\n\t"
    "v_max3_f32 v244, v244, v174, v175\n\t"
    "v_mov_b32 v247, v255\n\t"
    "v_mov_b32 v239, v248\n\t"
    "v_mov_b32 v238, v246\n\t"
    "v_mov_b32 v237, v245\n\t"
    "v_mov_b32 v233, v244\n\t"
    "v_permlane32_swap_b32 v255, v247\n\t"
    "v_permlane32_swap_b32 v248, v239\n\t"
    "v_permlane32_swap_b32 v246, v238\n\t"
    "v_permlane32_swap_b32 v245, v237\n\t"
    "v_permlane32_swap_b32 v244, v233\n\t"
    "v_max_f32 v255, v255, v247\n\t"
    "v_max_f32 v248, v248, v239\n\t"
    "v_max_f32 v246, v246, v238\n\t"
    "v_max_f32 v245, v245, v237\n\t"
    "v_max_f32 v244, v244, v233\n\t"
    "v_mov_b32 v247, v255\n\t"
    "v_mov_b32 v239, v248\n\t"
    "v_mov_b32 v238, v246\n\t"
    "v_mov_b32 v237, v245\n\t"
    "v_mov_b32 v233, v244\n\t"
    "v_permlane16_swap_b32 v255, v247\n\t"
    "s_add_u32 m0, s65, 16384\n\t"
    "s_mov_b32 s64, 16384\n\t"
    "buffer_load_dwordx4 v251, %[srdV], s64 offen offset:0 nt lds\n\t"
    "v_permlane16_swap_b32 v248, v239\n\t"
    "v_permlane16_swap_b32 v246, v238\n\t"
    "v_permlane16_swap_b32 v245, v237\n\t"
    "v_permlane16_swap_b32 v244, v233\n\t"
    "v_max_f32 v255, v255, v247\n\t"
    "v_max_f32 v248, v248, v239\n\t"
    "v_max_f32 v246, v246, v238\n\t"
    "v_max_f32 v245, v245, v237\n\t"
    "v_max_f32 v244, v244, v233\n\t"
    "ds_write_b32 v252, v255 offset:0\n\t"
    "ds_write_b32 v252, v248 offset:256\n\t"
    "ds_write_b32 v252, v246 offset:512\n\t"
    "ds_write_b32 v252, v245 offset:768\n\t"
    "ds_write_b32 v252, v244 offset:1024\n\t"
    "v_mov_b32 v36, 0x3f803f80\n\t"
    "v_mov_b32 v37, 0x3f803f80\n\t"
    "v_mov_b32 v38, 0x3f803f80\n\t"
    "v_mov_b32 v39, 0x3f803f80\n\t"
    "s_waitcnt lgkmcnt(0)\n\t"
    "s_barrier\n\t"
    "ds_read_b128 v[40:43], v234 offset:0\n\t"
    "ds_read_b128 v[44:47], v234 offset:256\n\t"
    "ds_read_b128 v[48:51], v234 offset:512\n\t"
    "ds_read_b128 v[176:179], v234 offset:768\n\t"
    "ds_read_b128 v[180:183], v234 offset:1024\n\t"
    "s_waitcnt lgkmcnt(4)\n\t"
    "v_max3_f32 v255, v40, v41, v42\n\t"
    "s_waitcnt lgkmcnt(3)\n\t"
    "v_max3_f32 v248, v44, v45, v46\n\t"
    "s_waitcnt lgkmcnt(2)\n\t"
    "v_max3_f32 v247, v48, v49, v50\n\t"
    "s_waitcnt lgkmcnt(1)\n\t"
    "v_max3_f32 v246, v176, v177, v178\n\t"
    "s_waitcnt lgkmcnt(0)\n\t"
    "v_max3_f32 v245, v180, v181, v182\n\t"
    "v_max_f32 v255, v255, v43\n\t"
    "v_max_f32 v248, v248, v47\n\t"
    "v_max_f32 v247, v247, v51\n\t"
    "v_max_f32 v246, v246, v179\n\t"
    "v_max_f32 v245, v245, v183\n\t"
    "v_mul_f32 v255, s79, v255\n\t"
    "v_mul_f32 v248, s79, v248\n\t"
    "v_mul_f32 v247, s79, v247\n\t"
    "v_mul_f32 v246, s79, v246\n\t"
    "v_mul_f32 v245, s79, v245\n\t"
    "v_fma_f32 v0, v0, s80, v255\n\t"
    "buffer_load_dwordx4 v250, %[srdV], s64 offen offset:1024 nt lds\n\t"
    "v_fma_f32 v1, v1, s80, v255\n\t"
    "buffer_load_dwordx4 v249, %[srdV], s64 offen offset:2048 nt lds\n\t"
    "v_fma_f32 v2, v2, s80, v255\n\t"
    "s_waitcnt vmcnt(15)\n\t"
    "ds_read_b64_tr_b16 v[40:41], v242 offset:0\n\t"
    "v_exp_f32 v0, v0\n\t"
    "ds_read_b64_tr_b16 v[42:43], v242 offset:2048\n\t"
    "v_fma_f32 v3, v3, s80, v255\n\t"
    "ds_read_b64_tr_b16 v[44:45], v241 offset:0\n\t"
    "v_exp_f32 v1, v1\n\t"
    "buffer_load_dwordx4 v243, %[srdV], s64 offen offset:3072 nt lds\n\t"
    "v_fma_f32 v52, v52, s80, v255\n\t"
    "ds_read_b64_tr_b16 v[46:47], v241 offset:2048\n\t"
    "v_exp_f32 v2, v2\n\t"
    "ds_read_b64_tr_b16 v[48:49], v240 offset:0\n\t"
    "v_fma_f32 v53, v53, s80, v255\n\t"
    "ds_read_b64_tr_b16 v[50:51], v240 offset:2048\n\t"
    "v_exp_f32 v3, v3\n\t"
    "ds_read_b64_tr_b16 v[176:177], v236 offset:0\n\t"
    "v_fma_f32 v54, v54, s80, v255\n\t"
    "ds_read_b64_tr_b16 v[178:179], v236 offset:2048\n\t"
    "v_exp_f32 v52, v52\n\t"
    "v_fma_f32 v55, v55, s80, v255\n\t"
    "v_exp_f32 v53, v53\n\t"
    "v_fma_f32 v4, v4, s80, v248\n\t"
    "v_exp_f32 v54, v54\n\t"
    "v_fma_f32 v5, v5, s80, v248\n\t"
    "v_exp_f32 v55, v55\n\t"
    "v_fma_f32 v6, v6, s80, v248\n\t"
    "v_exp_f32 v4, v4\n\t"
    "v_fma_f32 v7, v7, s80, v248\n\t"
    "v_exp_f32 v5, v5\n\t"
    "v_fma_f32 v56, v56, s80, v248\n\t"
    "v_cvt_pk_bf16_f32 v180, v0, v1\n\t"
    "s_add_u32 m0, s65, 0\n\t"
    "s_mov_b32 s64, 81920\n\t"
    "s_waitcnt lgkmcnt(0)\n\t"
    "buffer_load_dwordx4 v251, %[srdV], s64 offen offset:0 nt lds\n\t"
    "v_exp_f32 v6, v6\n\t"
    "v_fma_f32 v57, v57, s80, v248\n\t"
    "v_cvt_pk_bf16_f32 v181, v2, v3\n\t"
    "v_exp_f32 v7, v7\n\t"
    "v_fma_f32 v58, v58, s80, v248\n\t"
    "v_cvt_pk_bf16_f32 v182, v52, v53\n\t"
    "v_exp_f32 v56, v56\n\t"
    "v_fma_f32 v59, v59, s80, v248\n\t"
    "v_cvt_pk_bf16_f32 v183, v54, v55\n\t"
    "v_exp_f32 v57, v57\n\t"
    "v_fma_f32 v8, v8, s80, v247\n\t"
    "ds_write_b128 v235, v[180:183] offset:0\n\t"
    "v_exp_f32 v58, v58\n\t"
    "s_waitcnt vmcnt(13)\n\t"
    "ds_read_b64_tr_b16 v[0:1], v242 offset:4096\n\t"
    "v_fma_f32 v9, v9, s80, v247\n\t"
    "ds_read_b64_tr_b16 v[2:3], v242 offset:6144\n\t"
    "v_mfma_f32_16x16x32_bf16 v[52:55], v[36:39], v[180:183], 0\n\t"
    "buffer_load_dwordx4 v250, %[srdV], s64 offen offset:1024 nt lds\n\t"
    "v_exp_f32 v59, v59\n\t"
    "ds_read_b64_tr_b16 a[0:1], v241 offset:4096\n\t"
    "v_fma_f32 v10, v10, s80, v247\n\t"
    "ds_read_b64_tr_b16 a[2:3], v241 offset:6144\n\t"
    "v_exp_f32 v8, v8\n\t"
    "ds_read_b64_tr_b16 a[4:5], v240 offset:4096\n\t"
    "v_fma_f32 v11, v11, s80, v247\n\t"
    "ds_read_b64_tr_b16 a[6:7], v240 offset:6144\n\t"
    "v_exp_f32 v9, v9\n\t"
    "ds_read_b64_tr_b16 a[8:9], v236 offset:4096\n\t"
    "v_fma_f32 v60, v60, s80, v247\n\t"
    "ds_read_b64_tr_b16 a[10:11], v236 offset:6144\n\t"
    "v_cvt_pk_bf16_f32 v180, v4, v5\n\t"
    "v_exp_f32 v10, v10\n\t"
    "v_fma_f32 v61, v61, s80, v247\n\t"
    "v_cvt_pk_bf16_f32 v181, v6, v7\n\t"
    "v_exp_f32 v11, v11\n\t"
    "v_fma_f32 v62, v62, s80, v247\n\t"
    "v_cvt_pk_bf16_f32 v182, v56, v57\n\t"
    "v_exp_f32 v60, v60\n\t"
    "v_fma_f32 v63, v63, s80, v247\n\t"
    "v_cvt_pk_bf16_f32 v183, v58, v59\n\t"
    "v_exp_f32 v61, v61\n\t"
    "v_fma_f32 v20, v20, s80, v246\n\t"
    "buffer_load_dwordx4 v249, %[srdV], s64 offen offset:2048 nt lds\n\t"
    "ds_write_b128 v235, v[180:183] offset:1024\n\t"
    "v_exp_f32 v62, v62\n\t"
    "v_fma_f32 v21, v21, s80, v246\n\t"
    "v_mfma_f32_16x16x32_bf16 v[4:7], v[36:39], v[180:183], 0\n\t"
    "v_exp_f32 v63, v63\n\t"
    "v_fma_f32 v22, v22, s80, v246\n\t"
    "v_exp_f32 v20, v20\n\t"
    "v_fma_f32 v23, v23, s80, v246\n\t"
    "v_exp_f32 v21, v21\n\t"
    "v_fma_f32 v64, v64, s80, v246\n\t"
    "v_cvt_pk_bf16_f32 v56, v8, v9\n\t"
    "v_exp_f32 v22, v22\n\t"
    "v_fma_f32 v65, v65, s80, v246\n\t"
    "v_cvt_pk_bf16_f32 v57, v10, v11\n\t"
    "v_exp_f32 v23, v23\n\t"
    "buffer_load_dwordx4 v243, %[srdV], s64 offen offset:3072 nt lds\n\t"
    "v_fma_f32 v66, v66, s80, v246\n\t"
    "v_cvt_pk_bf16_f32 v58, v60, v61\n\t"
    "v_exp_f32 v64, v64\n\t"
    "v_fma_f32 v67, v67, s80, v246\n\t"
    "v_cvt_pk_bf16_f32 v59, v62, v63\n\t"
    "v_exp_f32 v65, v65\n\t"
    "v_fma_f32 v24, v24, s80, v245\n\t"
    "ds_write_b128 v235, v[56:59] offset:2048\n\t"
    "v_exp_f32 v66, v66\n\t"
    "v_fma_f32 v25, v25, s80, v245\n\t"
    "v_mfma_f32_16x16x32_bf16 v[8:11], v[36:39], v[56:59], 0\n\t"
    "v_exp_f32 v67, v67\n\t"
    "v_fma_f32 v26, v26, s80, v245\n\t"
    "v_exp_f32 v24, v24\n\t"
    "v_fma_f32 v27, v27, s80, v245\n\t"
    "v_exp_f32 v25, v25\n\t"
    "s_add_u32 m0, s65, 4096\n\t"
    "s_mov_b32 s64, 147456\n\t"
    "s_waitcnt lgkmcnt(2)\n\t"
    "buffer_load_dwordx4 v251, %[srdV], s64 offen offset:0 nt lds\n\t"
    "v_fma_f32 v68, v68, s80, v245\n\t"
    "v_cvt_pk_bf16_f32 v56, v20, v21\n\t"
    "v_exp_f32 v26, v26\n\t"
    "v_fma_f32 v69, v69, s80, v245\n\t"
    "v_cvt_pk_bf16_f32 v57, v22, v23\n\t"
    "v_exp_f32 v27, v27\n\t"
    "v_fma_f32 v70, v70, s80, v245\n\t"
    "v_cvt_pk_bf16_f32 v58, v64, v65\n\t"
    "v_exp_f32 v68, v68\n\t"
    "v_fma_f32 v71, v71, s80, v245\n\t"
    "v_cvt_pk_bf16_f32 v59, v66, v67\n\t"
    "v_exp_f32 v69, v69\n\t"
    "ds_write_b128 v235, v[56:59] offset:3072\n\t"
    "v_exp_f32 v70, v70\n\t"
    "v_mfma_f32_16x16x32_bf16 v[20:23], v[36:39], v[56:59], 0\n\t"
    "buffer_load_dwordx4 v250, %[srdV], s64 offen offset:1024 nt lds\n\t"
    "v_exp_f32 v71, v71\n\t"
    "v_cvt_pk_bf16_f32 v56, v24, v25\n\t"
    "v_cvt_pk_bf16_f32 v57, v26, v27\n\t"
    "v_cvt_pk_bf16_f32 v58, v68, v69\n\t"
    "v_cvt_pk_bf16_f32 v59, v70, v71\n\t"
    "ds_write_b128 v235, v[56:59] offset:4096\n\t"
    "s_nop 0\n\t"
    "v_mfma_f32_16x16x32_bf16 v[24:27], v[36:39], v[56:59], 0\n\t"
    "s_waitcnt lgkmcnt(0)\n\t"
    "s_barrier\n\t"
    "ds_read_b128 v[56:59], v254 offset:0\n\t"
    "ds_read_b128 v[60:63], v254 offset:1024\n\t"
    "ds_read_b128 v[64:67], v254 offset:2048\n\t"
    "ds_read_b128 v[68:71], v254 offset:3072\n\t"
    "ds_read_b128 a[12:15], v254 offset:4096\n\t"
    "s_waitcnt lgkmcnt(4)\n\t"
    "v_mfma_f32_16x16x32_bf16 v[180:183], v[40:43], v[56:59], 0\n\t"
    "buffer_load_dwordx4 v249, %[srdV], s64 offen offset:2048 nt lds\n\t"
    "ds_read_b128 a[16:19], v254 offset:20480\n\t"
    "s_waitcnt lgkmcnt(4)\n\t"
    "v_mfma_f32_16x16x32_bf16 v[184:187], v[40:43], v[60:63], 0\n\t"
    "buffer_load_dwordx4 v243, %[srdV], s64 offen offset:3072 nt lds\n\t"
    "s_waitcnt vmcnt(16)\n\t"
    "ds_read_b64_tr_b16 a[20:21], v242 offset:8192\n\t"
    "s_waitcnt lgkmcnt(4)\n\t"
    "v_mfma_f32_16x16x32_bf16 v[188:191], v[40:43], v[64:67], 0\n\t"
    "ds_read_b128 a[24:27], v254 offset:21504\n\t"
    "ds_read_b64_tr_b16 a[22:23], v242 offset:10240\n\t"
    "s_waitcnt lgkmcnt(5)\n\t"
    "v_mfma_f32_16x16x32_bf16 v[192:195], v[40:43], v[68:71], 0\n\t"
    "ds_read_b64_tr_b16 a[28:29], v241 offset:8192\n\t"
    "ds_read_b64_tr_b16 a[30:31], v241 offset:10240\n\t"
    "s_waitcnt lgkmcnt(6)\n\t"
    "v_mfma_f32_16x16x32_bf16 v[196:199], v[40:43], a[12:15], 0\n\t"
    "ds_read_b128 a[32:35], v254 offset:22528\n\t"
    "ds_read_b64_tr_b16 a[36:37], v240 offset:8192\n\t"
    "v_mfma_f32_16x16x32_bf16 v[40:43], v[44:47], v[56:59], 0\n\t"
    "ds_read_b64_tr_b16 a[38:39], v240 offset:10240\n\t"
    "ds_read_b64_tr_b16 a[40:41], v236 offset:8192\n\t"
    "v_mfma_f32_16x16x32_bf16 v[200:203], v[44:47], v[60:63], 0\n\t"
    "ds_read_b128 a[44:47], v254 offset:23552\n\t"
    "ds_read_b64_tr_b16 a[42:43], v236 offset:10240\n\t"
    "v_mfma_f32_16x16x32_bf16 v[204:207], v[44:47], v[64:67], 0\n\t"
    "v_fma_f32 v88, v88, s80, v255\n\t"
    "v_fma_f32 v89, v89, s80, v255\n\t"
    "v_mfma_f32_16x16x32_bf16 v[208:211], v[44:47], v[68:71], 0\n\t"
    "ds_read_b128 a[48:51], v254 offset:24576\n\t"
    "v_fma_f32 v90, v90, s80, v255\n\t"
    "v_mfma_f32_16x16x32_bf16 v[212:215], v[44:47], a[12:15], 0\n\t"
    "v_exp_f32 v88, v88\n\t"
    "v_mfma_f32_16x16x32_bf16 v[44:47], v[48:51], v[56:59], 0\n\t"
    "v_fma_f32 v91, v91, s80, v255\n\t"
    "v_mfma_f32_16x16x32_bf16 v[216:219], v[48:51], v[60:63], 0\n\t"
    "s_add_u32 m0, s65, 8192\n\t"
    "s_mov_b32 s64, 212992\n\t"
    "s_waitcnt lgkmcnt(1)\n\t"
    "buffer_load_dwordx4 v251, %[srdV], s64 offen offset:0 nt lds\n\t"
    "v_mfma_f32_16x16x32_bf16 v[220:223], v[48:51], v[64:67], 0\n\t"
    "v_exp_f32 v89, v89\n\t"
    "v_mfma_f32_16x16x32_bf16 v[224:227], v[48:51], v[68:71], 0\n\t"
    "v_fma_f32 v12, v12, s80, v255\n\t"
    "v_mfma_f32_16x16x32_bf16 v[228:231], v[48:51], a[12:15], 0\n\t"
    "v_exp_f32 v90, v90\n\t"
    "v_mfma_f32_16x16x32_bf16 v[48:51], v[176:179], v[56:59], 0\n\t"
    "v_fma_f32 v13, v13, s80, v255\n\t"
    "v_mfma_f32_16x16x32_bf16 v[56:59], v[176:179], v[60:63], 0\n\t"
    "buffer_load_dwordx4 v250, %[srdV], s64 offen offset:1024 nt lds\n\t"
    "v_mfma_f32_16x16x32_bf16 v[60:63], v[176:179], v[64:67], 0\n\t"
    "v_exp_f32 v91, v91\n\t"
    "v_mfma_f32_16x16x32_bf16 v[64:67], v[176:179], v[68:71], 0\n\t"
    "v_fma_f32 v14, v14, s80, v255\n\t"
    "v_mfma_f32_16x16x32_bf16 v[68:71], v[176:179], a[12:15], 0\n\t"
    "v_exp_f32 v12, v12\n\t"
    "v_mfma_f32_16x16x32_bf16 v[180:183], v[0:3], a[16:19], v[180:183]\n\t"
    "ds_read_b128 v[176:179], v253 offset:0\n\t"
    "s_waitcnt vmcnt(14)\n\t"
    "ds_read_b64_tr_b16 a[12:13], v242 offset:12288\n\t"
    "v_mfma_f32_16x16x32_bf16 v[184:187], v[0:3], a[24:27], v[184:187]\n\t"
    "ds_read_b64_tr_b16 a[14:15], v242 offset:14336\n\t"
    "ds_read_b64_tr_b16 a[52:53], v241 offset:12288\n\t"
    "v_mfma_f32_16x16x32_bf16 v[188:191], v[0:3], a[32:35], v[188:191]\n\t"
    "ds_read_b128 a[56:59], v253 offset:1024\n\t"
    "ds_read_b64_tr_b16 a[54:55], v241 offset:14336\n\t"
    "v_mfma_f32_16x16x32_bf16 v[192:195], v[0:3], a[44:47], v[192:195]\n\t"
    "ds_read_b64_tr_b16 a[60:61], v240 offset:12288\n\t"
    "ds_read_b64_tr_b16 a[62:63], v240 offset:14336\n\t"
    "s_waitcnt lgkmcnt(8)\n\t"
    "v_mfma_f32_16x16x32_bf16 v[196:199], v[0:3], a[48:51], v[196:199]\n\t"
    "ds_read_b128 a[64:67], v253 offset:2048\n\t"
    "ds_read_b64_tr_b16 a[68:69], v236 offset:12288\n\t"
    "v_mfma_f32_16x16x32_bf16 v[40:43], a[0:3], a[16:19], v[40:43]\n\t"
    "buffer_load_dwordx4 v249, %[srdV], s64 offen offset:2048 nt lds\n\t"
    "ds_read_b64_tr_b16 a[70:71], v236 offset:14336\n\t"
    "v_mfma_f32_16x16x32_bf16 v[200:203], a[0:3], a[24:27], v[200:203]\n\t"
    "ds_read_b128 a[72:75], v253 offset:3072\n\t"
    "v_fma_f32 v15, v15, s80, v255\n\t"
    "v_mfma_f32_16x16x32_bf16 v[204:207], a[0:3], a[32:35], v[204:207]\n\t"
    "v_exp_f32 v13, v13\n\t"
    "v_mfma_f32_16x16x32_bf16 v[208:211], a[0:3], a[44:47], v[208:211]\n\t"
    "ds_read_b128 a[76:79], v253 offset:4096\n\t"
    "v_fma_f32 v92, v92, s80, v248\n\t"
    "v_mfma_f32_16x16x32_bf16 v[212:215], a[0:3], a[48:51], v[212:215]\n\t"
    "v_exp_f32 v14, v14\n\t"
    "v_mfma_f32_16x16x32_bf16 v[44:47], a[4:7], a[16:19], v[44:47]\n\t"
    "v_fma_f32 v93, v93, s80, v248\n\t"
    "v_mfma_f32_16x16x32_bf16 v[216:219], a[4:7], a[24:27], v[216:219]\n\t"
    "v_exp_f32 v15, v15\n\t"
    "v_mfma_f32_16x16x32_bf16 v[220:223], a[4:7], a[32:35], v[220:223]\n\t"
    "v_fma_f32 v94, v94, s80, v248\n\t"
    "v_mfma_f32_16x16x32_bf16 v[224:227], a[4:7], a[44:47], v[224:227]\n\t"
    "buffer_load_dwordx4 v243, %[srdV], s64 offen offset:3072 nt lds\n\t"
    "v_mfma_f32_16x16x32_bf16 v[228:231], a[4:7], a[48:51], v[228:231]\n\t"
    "v_exp_f32 v92, v92\n\t"
    "v_mfma_f32_16x16x32_bf16 v[48:51], a[8:11], a[16:19], v[48:51]\n\t"
    "v_fma_f32 v95, v95, s80, v248\n\t"
    "v_mfma_f32_16x16x32_bf16 v[56:59], a[8:11], a[24:27], v[56:59]\n\t"
    "v_exp_f32 v93, v93\n\t"
    "v_mfma_f32_16x16x32_bf16 v[60:63], a[8:11], a[32:35], v[60:63]\n\t"
    "v_fma_f32 v16, v16, s80, v248\n\t"
    "v_cvt_pk_bf16_f32 v0, v88, v89\n\t"
    "v_mfma_f32_16x16x32_bf16 v[64:67], a[8:11], a[44:47], v[64:67]\n\t"
    "v_exp_f32 v94, v94\n\t"
    "v_mfma_f32_16x16x32_bf16 v[68:71], a[8:11], a[48:51], v[68:71]\n\t"
    "v_fma_f32 v17, v17, s80, v248\n\t"
    "v_cvt_pk_bf16_f32 v1, v90, v91\n\t"
    "s_waitcnt lgkmcnt(12)\n\t"
    "v_mfma_f32_16x16x32_bf16 v[180:183], a[20:23], v[176:179], v[180:183]\n\t"
    "ds_read_b128 v[88:91], v253 offset:20480\n\t"
    "s_waitcnt lgkmcnt(9)\n\t"
    "v_mfma_f32_16x16x32_bf16 v[184:187], a[20:23], a[56:59], v[184:187]\n\t"
    "v_exp_f32 v95, v95\n\t"
    "s_waitcnt lgkmcnt(5)\n\t"
    "v_mfma_f32_16x16x32_bf16 v[188:191], a[20:23], a[64:67], v[188:191]\n\t"
    "s_add_u32 m0, s65, 12288\n\t"
    "s_mov_b32 s64, 32768\n\t"
    "s_waitcnt lgkmcnt(3)\n\t"
    "buffer_load_dwordx4 v251, %[srdV], s64 offen offset:0 nt lds\n\t"
    "s_waitcnt lgkmcnt(2)\n\t"
    "v_mfma_f32_16x16x32_bf16 v[192:195], a[20:23], a[72:75], v[192:195]\n\t"
    "ds_read_b128 a[0:3], v253 offset:21504\n\t"
    "v_fma_f32 v18, v18, s80, v248\n\t"
    "s_waitcnt lgkmcnt(2)\n\t"
    "v_mfma_f32_16x16x32_bf16 v[196:199], a[20:23], a[76:79], v[196:199]\n\t"
    "v_cvt_pk_bf16_f32 v2, v12, v13\n\t"
    "v_mfma_f32_16x16x32_bf16 v[40:43], a[28:31], v[176:179], v[40:43]\n\t"
    "ds_read_b128 a[4:7], v253 offset:22528\n\t"
    "v_mfma_f32_16x16x32_bf16 v[200:203], a[28:31], a[56:59], v[200:203]\n\t"
    "v_exp_f32 v16, v16\n\t"
    "v_mfma_f32_16x16x32_bf16 v[204:207], a[28:31], a[64:67], v[204:207]\n\t"
    "ds_read_b128 a[8:11], v253 offset:23552\n\t"
    "s_waitcnt vmcnt(13)\n\t"
    "ds_read_b64_tr_b16 a[16:17], v242 offset:16384\n\t"
    "v_mfma_f32_16x16x32_bf16 v[208:211], a[28:31], a[72:75], v[208:211]\n\t"
    "ds_read_b64_tr_b16 a[18:19], v242 offset:18432\n\t"
    "ds_read_b64_tr_b16 a[20:21], v241 offset:16384\n\t"
    "v_mfma_f32_16x16x32_bf16 v[212:215], a[28:31], a[76:79], v[212:215]\n\t"
    "ds_read_b128 a[24:27], v253 offset:24576\n\t"
    "ds_read_b64_tr_b16 a[22:23], v241 offset:18432\n\t"
    "v_mfma_f32_16x16x32_bf16 v[44:47], a[36:39], v[176:179], v[44:47]\n\t"
    "buffer_load_dwordx4 v250, %[srdV], s64 offen offset:1024 nt lds\n\t"
    "ds_read_b64_tr_b16 a[28:29], v240 offset:16384\n\t"
    "v_mfma_f32_16x16x32_bf16 v[216:219], a[36:39], a[56:59], v[216:219]\n\t"
    "ds_read_b64_tr_b16 a[30:31], v240 offset:18432\n\t"
    "ds_read_b64_tr_b16 a[32:33], v236 offset:16384\n\t"
    "v_mfma_f32_16x16x32_bf16 v[220:223], a[36:39], a[64:67], v[220:223]\n\t"
    "ds_read_b64_tr_b16 a[34:35], v236 offset:18432\n\t"
    "v_fma_f32 v19, v19, s80, v248\n\t"
    "v_mfma_f32_16x16x32_bf16 v[224:227], a[36:39], a[72:75], v[224:227]\n\t"
    "v_cvt_pk_bf16_f32 v3, v14, v15\n\t"
    "v_mfma_f32_16x16x32_bf16 v[228:231], a[36:39], a[76:79], v[228:231]\n\t"
    "v_exp_f32 v17, v17\n\t"
    "v_mfma_f32_16x16x32_bf16 v[48:51], a[40:43], v[176:179], v[48:51]\n\t"
    "v_fma_f32 v96, v96, s80, v247\n\t"
    "ds_write_b128 v235, v[0:3] offset:5120\n\t"
    "v_mfma_f32_16x16x32_bf16 v[56:59], a[40:43], a[56:59], v[56:59]\n\t"
    "v_exp_f32 v18, v18\n\t"
    "v_mfma_f32_16x16x32_bf16 v[60:63], a[40:43], a[64:67], v[60:63]\n\t"
    "v_fma_f32 v97, v97, s80, v247\n\t"
    "v_mfma_f32_16x16x32_bf16 v[64:67], a[40:43], a[72:75], v[64:67]\n\t"
    "v_mfma_f32_16x16x32_bf16 v[52:55], v[36:39], v[0:3], v[52:55]\n\t"
    "v_mfma_f32_16x16x32_bf16 v[68:71], a[40:43], a[76:79], v[68:71]\n\t"
    "buffer_load_dwordx4 v249, %[srdV], s64 offen offset:2048 nt lds\n\t"
    "s_waitcnt lgkmcnt(13)\n\t"
    "v_mfma_f32_16x16x32_bf16 v[180:183], a[12:15], v[88:91], v[180:183]\n\t"
    "v_exp_f32 v19, v19\n\t"
    "s_waitcnt lgkmcnt(12)\n\t"
    "v_mfma_f32_16x16x32_bf16 v[184:187], a[12:15], a[0:3], v[184:187]\n\t"
    "v_fma_f32 v98, v98, s80, v247\n\t"
    "s_waitcnt lgkmcnt(11)\n\t"
    "v_mfma_f32_16x16x32_bf16 v[188:191], a[12:15], a[4:7], v[188:191]\n\t"
    "v_exp_f32 v96, v96\n\t"
    "s_waitcnt lgkmcnt(10)\n\t"
    "v_mfma_f32_16x16x32_bf16 v[192:195], a[12:15], a[8:11], v[192:195]\n\t"
    "v_fma_f32 v99, v99, s80, v247\n\t"
    "s_waitcnt lgkmcnt(6)\n\t"
    "v_mfma_f32_16x16x32_bf16 v[196:199], a[12:15], a[24:27], v[196:199]\n\t"
    "v_exp_f32 v97, v97\n\t"
    "v_mfma_f32_16x16x32_bf16 v[40:43], a[52:55], v[88:91], v[40:43]\n\t"
    "v_fma_f32 v28, v28, s80, v247\n\t"
    "v_cvt_pk_bf16_f32 v0, v92, v93\n\t"
    "v_mfma_f32_16x16x32_bf16 v[200:203], a[52:55], a[0:3], v[200:203]\n\t"
    "v_exp_f32 v98, v98\n\t"
    "v_mfma_f32_16x16x32_bf16 v[204:207], a[52:55], a[4:7], v[204:207]\n\t"
    "buffer_load_dwordx4 v243, %[srdV], s64 offen offset:3072 nt lds\n\t"
    "v_fma_f32 v29, v29, s80, v247\n\t"
    "v_mfma_f32_16x16x32_bf16 v[208:211], a[52:55], a[8:11], v[208:211]\n\t"
    "v_cvt_pk_bf16_f32 v1, v94, v95\n\t"
    "v_mfma_f32_16x16x32_bf16 v[212:215], a[52:55], a[24:27], v[212:215]\n\t"
    "v_exp_f32 v99, v99\n\t"
    "v_mfma_f32_16x16x32_bf16 v[44:47], a[60:63], v[88:91], v[44:47]\n\t"
    "v_fma_f32 v30, v30, s80, v247\n\t"
    "v_cvt_pk_bf16_f32 v2, v16, v17\n\t"
    "v_mfma_f32_16x16x32_bf16 v[216:219], a[60:63], a[0:3], v[216:219]\n\t"
    "v_exp_f32 v28, v28\n\t"
    "v_mfma_f32_16x16x32_bf16 v[220:223], a[60:63], a[4:7], v[220:223]\n\t"
    "v_fma_f32 v31, v31, s80, v247\n\t"
    "v_cvt_pk_bf16_f32 v3, v18, v19\n\t"
    "v_mfma_f32_16x16x32_bf16 v[224:227], a[60:63], a[8:11], v[224:227]\n\t"
    "v_exp_f32 v29, v29\n\t"
    "v_mfma_f32_16x16x32_bf16 v[228:231], a[60:63], a[24:27], v[228:231]\n\t"
    "v_fma_f32 v100, v100, s80, v246\n\t"
    "ds_write_b128 v235, v[0:3] offset:6144\n\t"
    "v_mfma_f32_16x16x32_bf16 v[48:51], a[68:71], v[88:91], v[48:51]\n\t"
    "v_exp_f32 v30, v30\n\t"
    "v_mfma_f32_16x16x32_bf16 v[56:59], a[68:71], a[0:3], v[56:59]\n\t"
    "s_add_u32 m0, s65, 16384\n\t"
    "s_mov_b32 s64, 98304\n\t"
    "s_waitcnt lgkmcnt(2)\n\t"
    "buffer_load_dwordx4 v251, %[srdV], s64 offen offset:0 nt lds\n\t"
    "v_mfma_f32_16x16x32_bf16 v[60:63], a[68:71], a[4:7], v[60:63]\n\t"
    "v_fma_f32 v101, v101, s80, v246\n\t"
    "v_mfma_f32_16x16x32_bf16 v[64:67], a[68:71], a[8:11], v[64:67]\n\t"
    "v_mfma_f32_16x16x32_bf16 v[4:7], v[36:39], v[0:3], v[4:7]\n\t"
    "v_mfma_f32_16x16x32_bf16 v[68:71], a[68:71], a[24:27], v[68:71]\n\t"
    "v_exp_f32 v31, v31\n\t"
    "v_fma_f32 v102, v102, s80, v246\n\t"
    "v_exp_f32 v100, v100\n\t"
    "v_fma_f32 v103, v103, s80, v246\n\t"
    "v_exp_f32 v101, v101\n\t"
    "v_fma_f32 v128, v128, s80, v246\n\t"
    "v_cvt_pk_bf16_f32 v0, v96, v97\n\t"
    "v_exp_f32 v102, v102\n\t"
    "v_fma_f32 v129, v129, s80, v246\n\t"
    "v_cvt_pk_bf16_f32 v1, v98, v99\n\t"
    "v_exp_f32 v103, v103\n\t"
    "v_fma_f32 v130, v130, s80, v246\n\t"
    "v_cvt_pk_bf16_f32 v2, v28, v29\n\t"
    "v_exp_f32 v128, v128\n\t"
    "v_fma_f32 v131, v131, s80, v246\n\t"
    "v_cvt_pk_bf16_f32 v3, v30, v31\n\t"
    "v_exp_f32 v129, v129\n\t"
    "v_fma_f32 v104, v104, s80, v245\n\t"
    "ds_write_b128 v235, v[0:3] offset:7168\n\t"
    "v_exp_f32 v130, v130\n\t"
    "v_fma_f32 v105, v105, s80, v245\n\t"
    "v_mfma_f32_16x16x32_bf16 v[8:11], v[36:39], v[0:3], v[8:11]\n\t"
    "v_exp_f32 v131, v131\n\t"
    "v_fma_f32 v106, v106, s80, v245\n\t"
    "v_exp_f32 v104, v104\n\t"
    "v_fma_f32 v107, v107, s80, v245\n\t"
    "v_exp_f32 v105, v105\n\t"
    "v_fma_f32 v140, v140, s80, v245\n\t"
    "v_cvt_pk_bf16_f32 v0, v100, v101\n\t"
    "v_exp_f32 v106, v106\n\t"
    "v_fma_f32 v141, v141, s80, v245\n\t"
    "v_cvt_pk_bf16_f32 v1, v102, v103\n\t"
    "v_exp_f32 v107, v107\n\t"
    "v_fma_f32 v142, v142, s80, v245\n\t"
    "v_cvt_pk_bf16_f32 v2, v128, v129\n\t"
    "v_exp_f32 v140, v140\n\t"
    "v_fma_f32 v143, v143, s80, v245\n\t"
    "v_cvt_pk_bf16_f32 v3, v130, v131\n\t"
    "v_exp_f32 v141, v141\n\t"
    "ds_write_b128 v235, v[0:3] offset:8192\n\t"
    "v_exp_f32 v142, v142\n\t"
    "v_mfma_f32_16x16x32_bf16 v[20:23], v[36:39], v[0:3], v[20:23]\n\t"
    "v_exp_f32 v143, v143\n\t"
    "v_cvt_pk_bf16_f32 v0, v104, v105\n\t"
    "v_cvt_pk_bf16_f32 v1, v106, v107\n\t"
    "v_cvt_pk_bf16_f32 v2, v140, v141\n\t"
    "v_cvt_pk_bf16_f32 v3, v142, v143\n\t"
    "ds_write_b128 v235, v[0:3] offset:9216\n\t"
    "s_nop 0\n\t"
    "v_mfma_f32_16x16x32_bf16 v[24:27], v[36:39], v[0:3], v[24:27]\n\t"
    "s_waitcnt lgkmcnt(0)\n\t"
    "s_barrier\n\t"
    "ds_read_b128 v[0:3], v254 offset:5120\n\t"
    "ds_read_b128 v[12:15], v254 offset:6144\n\t"
    "ds_read_b128 v[16:19], v254 offset:7168\n\t"
    "ds_read_b128 v[28:31], v254 offset:8192\n\t"
    "ds_read_b128 v[88:91], v254 offset:9216\n\t"
    "s_waitcnt lgkmcnt(4)\n\t"
    "v_mfma_f32_16x16x32_bf16 v[180:183], a[16:19], v[0:3], v[180:183]\n\t"
    "buffer_load_dwordx4 v250, %[srdV], s64 offen offset:1024 nt lds\n\t"
    "ds_read_b128 v[92:95], v254 offset:25600\n\t"
    "s_waitcnt lgkmcnt(4)\n\t"
    "v_mfma_f32_16x16x32_bf16 v[184:187], a[16:19], v[12:15], v[184:187]\n\t"
    "buffer_load_dwordx4 v249, %[srdV], s64 offen offset:2048 nt lds\n\t"
    "s_waitcnt vmcnt(15)\n\t"
    "ds_read_b64_tr_b16 v[96:97], v242 offset:0\n\t"
    "s_waitcnt lgkmcnt(4)\n\t"
    "v_mfma_f32_16x16x32_bf16 v[188:191], a[16:19], v[16:19], v[188:191]\n\t"
    "ds_read_b128 v[100:103], v254 offset:26624\n\t"
    "ds_read_b64_tr_b16 v[98:99], v242 offset:2048\n\t"
    "s_waitcnt lgkmcnt(5)\n\t"
    "v_mfma_f32_16x16x32_bf16 v[192:195], a[16:19], v[28:31], v[192:195]\n\t"
    "buffer_load_dwordx4 v243, %[srdV], s64 offen offset:3072 nt lds\n\t"
    "ds_read_b64_tr_b16 v[104:105], v241 offset:0\n\t"
    "s_waitcnt lgkmcnt(5)\n\t"
    "v_mfma_f32_16x16x32_bf16 v[196:199], a[16:19], v[88:91], v[196:199]\n\t"
    "ds_read_b128 v[128:131], v254 offset:27648\n\t"
    "ds_read_b64_tr_b16 v[106:107], v241 offset:2048\n\t"
    "v_mfma_f32_16x16x32_bf16 v[40:43], a[20:23], v[0:3], v[40:43]\n\t"
    "ds_read_b64_tr_b16 v[140:141], v240 offset:0\n\t"
    "ds_read_b64_tr_b16 v[142:143], v240 offset:2048\n\t"
    "v_mfma_f32_16x16x32_bf16 v[200:203], a[20:23], v[12:15], v[200:203]\n\t"
    "ds_read_b128 v[176:179], v254 offset:28672\n\t"
    "ds_read_b64_tr_b16 a[0:1], v236 offset:0\n\t"
    "v_mfma_f32_16x16x32_bf16 v[204:207], a[20:23], v[16:19], v[204:207]\n\t"
    "ds_read_b64_tr_b16 a[2:3], v236 offset:2048\n\t"
    "v_fma_f32 v72, v72, s80, v255\n\t"
    "v_mfma_f32_16x16x32_bf16 v[208:211], a[20:23], v[28:31], v[208:211]\n\t"
    "ds_read_b128 a[4:7], v254 offset:29696\n\t"
    "v_fma_f32 v73, v73, s80, v255\n\t"
    "v_mfma_f32_16x16x32_bf16 v[212:215], a[20:23], v[88:91], v[212:215]\n\t"
    "v_fma_f32 v74, v74, s80, v255\n\t"
    "v_mfma_f32_16x16x32_bf16 v[44:47], a[28:31], v[0:3], v[44:47]\n\t"
    "v_exp_f32 v72, v72\n\t"
    "v_mfma_f32_16x16x32_bf16 v[216:219], a[28:31], v[12:15], v[216:219]\n\t"
    "v_fma_f32 v75, v75, s80, v255\n\t"
    "v_mfma_f32_16x16x32_bf16 v[220:223], a[28:31], v[16:19], v[220:223]\n\t"
    "s_add_u32 m0, s65, 0\n\t"
    "s_mov_b32 s64, 163840\n\t"
    "s_waitcnt lgkmcnt(1)\n\t"
    "buffer_load_dwordx4 v251, %[srdV], s64 offen offset:0 nt lds\n\t"
    "v_mfma_f32_16x16x32_bf16 v[224:227], a[28:31], v[28:31], v[224:227]\n\t"
    "v_exp_f32 v73, v73\n\t"
    "v_mfma_f32_16x16x32_bf16 v[228:231], a[28:31], v[88:91], v[228:231]\n\t"
    "v_fma_f32 v120, v120, s80, v255\n\t"
    "v_mfma_f32_16x16x32_bf16 v[48:51], a[32:35], v[0:3], v[48:51]\n\t"
    "v_exp_f32 v74, v74\n\t"
    "v_mfma_f32_16x16x32_bf16 v[56:59], a[32:35], v[12:15], v[56:59]\n\t"
    "v_fma_f32 v121, v121, s80, v255\n\t"
    "v_mfma_f32_16x16x32_bf16 v[60:63], a[32:35], v[16:19], v[60:63]\n\t"
    "v_exp_f32 v75, v75\n\t"
    "v_mfma_f32_16x16x32_bf16 v[64:67], a[32:35], v[28:31], v[64:67]\n\t"
    "v_fma_f32 v122, v122, s80, v255\n\t"
    "v_mfma_f32_16x16x32_bf16 v[68:71], a[32:35], v[88:91], v[68:71]\n\t"
    "v_exp_f32 v120, v120\n\t"
    "v_mfma_f32_16x16x32_bf16 v[180:183], v[96:99], v[92:95], v[180:183]\n\t"
    "buffer_load_dwordx4 v250, %[srdV], s64 offen offset:1024 nt lds\n\t"
    "ds_read_b128 v[0:3], v253 offset:5120\n\t"
    "v_mfma_f32_16x16x32_bf16 v[184:187], v[96:99], v[100:103], v[184:187]\n\t"
    "s_waitcnt vmcnt(14)\n\t"
    "ds_read_b64_tr_b16 v[12:13], v242 offset:4096\n\t"
    "ds_read_b64_tr_b16 v[14:15], v242 offset:6144\n\t"
    "v_mfma_f32_16x16x32_bf16 v[188:191], v[96:99], v[128:131], v[188:191]\n\t"
    "ds_read_b128 v[16:19], v253 offset:6144\n\t"
    "ds_read_b64_tr_b16 v[28:29], v241 offset:4096\n\t"
    "v_mfma_f32_16x16x32_bf16 v[192:195], v[96:99], v[176:179], v[192:195]\n\t"
    "ds_read_b64_tr_b16 v[30:31], v241 offset:6144\n\t"
    "ds_read_b64_tr_b16 v[88:89], v240 offset:4096\n\t"
    "s_waitcnt lgkmcnt(7)\n\t"
    "v_mfma_f32_16x16x32_bf16 v[196:199], v[96:99], a[4:7], v[196:199]\n\t"
    "ds_read_b128 v[96:99], v253 offset:7168\n\t"
    "ds_read_b64_tr_b16 v[90:91], v240 offset:6144\n\t"
    "v_mfma_f32_16x16x32_bf16 v[40:43], v[104:107], v[92:95], v[40:43]\n\t"
    "ds_read_b64_tr_b16 a[8:9], v236 offset:4096\n\t"
    "ds_read_b64_tr_b16 a[10:11], v236 offset:6144\n\t"
    "v_mfma_f32_16x16x32_bf16 v[200:203], v[104:107], v[100:103], v[200:203]\n\t"
    "ds_read_b128 a[12:15], v253 offset:8192\n\t"
    "v_fma_f32 v123, v123, s80, v255\n\t"
    "v_mfma_f32_16x16x32_bf16 v[204:207], v[104:107], v[128:131], v[204:207]\n\t"
    "v_exp_f32 v121, v121\n\t"
    "v_mfma_f32_16x16x32_bf16 v[208:211], v[104:107], v[176:179], v[208:211]\n\t"
    "ds_read_b128 a[16:19], v253 offset:9216\n\t"
    "v_fma_f32 v76, v76, s80, v248\n\t"
    "v_mfma_f32_16x16x32_bf16 v[212:215], v[104:107], a[4:7], v[212:215]\n\t"
    "buffer_load_dwordx4 v249, %[srdV], s64 offen offset:2048 nt lds\n\t"
    "v_mfma_f32_16x16x32_bf16 v[44:47], v[140:143], v[92:95], v[44:47]\n\t"
    "v_exp_f32 v122, v122\n\t"
    "v_mfma_f32_16x16x32_bf16 v[216:219], v[140:143], v[100:103], v[216:219]\n\t"
    "v_fma_f32 v77, v77, s80, v248\n\t"
    "v_mfma_f32_16x16x32_bf16 v[220:223], v[140:143], v[128:131], v[220:223]\n\t"
    "v_exp_f32 v123, v123\n\t"
    "v_mfma_f32_16x16x32_bf16 v[224:227], v[140:143], v[176:179], v[224:227]\n\t"
    "v_fma_f32 v78, v78, s80, v248\n\t"
    "v_mfma_f32_16x16x32_bf16 v[228:231], v[140:143], a[4:7], v[228:231]\n\t"
    "v_exp_f32 v76, v76\n\t"
    "v_mfma_f32_16x16x32_bf16 v[48:51], a[0:3], v[92:95], v[48:51]\n\t"
    "v_fma_f32 v79, v79, s80, v248\n\t"
    "v_mfma_f32_16x16x32_bf16 v[56:59], a[0:3], v[100:103], v[56:59]\n\t"
    "v_exp_f32 v77, v77\n\t"
    "v_mfma_f32_16x16x32_bf16 v[60:63], a[0:3], v[128:131], v[60:63]\n\t"
    "buffer_load_dwordx4 v243, %[srdV], s64 offen offset:3072 nt lds\n\t"
    "v_fma_f32 v124, v124, s80, v248\n\t"
    "v_mfma_f32_16x16x32_bf16 v[64:67], a[0:3], v[176:179], v[64:67]\n\t"
    "v_cvt_pk_bf16_f32 v92, v72, v73\n\t"
    "v_mfma_f32_16x16x32_bf16 v[68:71], a[0:3], a[4:7], v[68:71]\n\t"
    "v_exp_f32 v78, v78\n\t"
    "s_waitcnt lgkmcnt(10)\n\t"
    "v_mfma_f32_16x16x32_bf16 v[180:183], v[12:15], v[0:3], v[180:183]\n\t"
    "ds_read_b128 v[100:103], v253 offset:25600\n\t"
    "v_fma_f32 v125, v125, s80, v248\n\t"
    "s_waitcnt lgkmcnt(10)\n\t"
    "v_mfma_f32_16x16x32_bf16 v[184:187], v[12:15], v[16:19], v[184:187]\n\t"
    "v_cvt_pk_bf16_f32 v93, v74, v75\n\t"
    "s_waitcnt lgkmcnt(6)\n\t"
    "v_mfma_f32_16x16x32_bf16 v[188:191], v[12:15], v[96:99], v[188:191]\n\t"
    "ds_read_b128 v[72:75], v253 offset:26624\n\t"
    "s_waitcnt lgkmcnt(3)\n\t"
    "v_mfma_f32_16x16x32_bf16 v[192:195], v[12:15], a[12:15], v[192:195]\n\t"
    "v_exp_f32 v79, v79\n\t"
    "s_waitcnt lgkmcnt(2)\n\t"
    "v_mfma_f32_16x16x32_bf16 v[196:199], v[12:15], a[16:19], v[196:199]\n\t"
    "ds_read_b128 v[12:15], v253 offset:27648\n\t"
    "v_fma_f32 v126, v126, s80, v248\n\t"
    "v_mfma_f32_16x16x32_bf16 v[40:43], v[28:31], v[0:3], v[40:43]\n\t"
    "v_cvt_pk_bf16_f32 v94, v120, v121\n\t"
    "v_mfma_f32_16x16x32_bf16 v[200:203], v[28:31], v[16:19], v[200:203]\n\t"
    "s_add_u32 m0, s65, 4096\n\t"
    "s_mov_b32 s64, 229376\n\t"
    "buffer_load_dwordx4 v251, %[srdV], s64 offen offset:0 nt lds\n\t"
    "v_mfma_f32_16x16x32_bf16 v[204:207], v[28:31], v[96:99], v[204:207]\n\t"
    "ds_read_b128 v[104:107], v253 offset:28672\n\t"
    "v_mfma_f32_16x16x32_bf16 v[208:211], v[28:31], a[12:15], v[208:211]\n\t"
    "v_exp_f32 v124, v124\n\t"
    "v_mfma_f32_16x16x32_bf16 v[212:215], v[28:31], a[16:19], v[212:215]\n\t"
    "ds_read_b128 v[28:31], v253 offset:29696\n\t"
    "v_fma_f32 v127, v127, s80, v248\n\t"
    "v_mfma_f32_16x16x32_bf16 v[44:47], v[88:91], v[0:3], v[44:47]\n\t"
    "v_cvt_pk_bf16_f32 v95, v122, v123\n\t"
    "v_mfma_f32_16x16x32_bf16 v[216:219], v[88:91], v[16:19], v[216:219]\n\t"
    "s_waitcnt vmcnt(13)\n\t"
    "ds_read_b64_tr_b16 v[120:121], v242 offset:8192\n\t"
    "ds_read_b64_tr_b16 v[122:123], v242 offset:10240\n\t"
    "v_mfma_f32_16x16x32_bf16 v[220:223], v[88:91], v[96:99], v[220:223]\n\t"
    "ds_read_b64_tr_b16 v[128:129], v241 offset:8192\n\t"
    "ds_read_b64_tr_b16 v[130:131], v241 offset:10240\n\t"
    "v_mfma_f32_16x16x32_bf16 v[224:227], v[88:91], a[12:15], v[224:227]\n\t"
    "ds_read_b64_tr_b16 v[140:141], v240 offset:8192\n\t"
    "ds_read_b64_tr_b16 v[142:143], v240 offset:10240\n\t"
    "v_mfma_f32_16x16x32_bf16 v[228:231], v[88:91], a[16:19], v[228:231]\n\t"
    "buffer_load_dwordx4 v250, %[srdV], s64 offen offset:1024 nt lds\n\t"
    "ds_read_b64_tr_b16 v[88:89], v236 offset:8192\n\t"
    "v_mfma_f32_16x16x32_bf16 v[48:51], a[8:11], v[0:3], v[48:51]\n\t"
    "ds_read_b64_tr_b16 v[90:91], v236 offset:10240\n\t"
    "v_mfma_f32_16x16x32_bf16 v[56:59], a[8:11], v[16:19], v[56:59]\n\t"
    "v_exp_f32 v125, v125\n\t"
    "v_mfma_f32_16x16x32_bf16 v[60:63], a[8:11], v[96:99], v[60:63]\n\t"
    "v_fma_f32 v80, v80, s80, v247\n\t"
    "ds_write_b128 v235, v[92:95] offset:10240\n\t"
    "v_mfma_f32_16x16x32_bf16 v[64:67], a[8:11], a[12:15], v[64:67]\n\t"
    "v_exp_f32 v126, v126\n\t"
    "v_mfma_f32_16x16x32_bf16 v[68:71], a[8:11], a[16:19], v[68:71]\n\t"
    "v_fma_f32 v81, v81, s80, v247\n\t"
    "s_waitcnt lgkmcnt(7)\n\t"
    "v_mfma_f32_16x16x32_bf16 v[180:183], v[120:123], v[100:103], v[180:183]\n\t"
    "v_mfma_f32_16x16x32_bf16 v[52:55], v[36:39], v[92:95], v[52:55]\n\t"
    "v_mfma_f32_16x16x32_bf16 v[184:187], v[120:123], v[72:75], v[184:187]\n\t"
    "v_exp_f32 v127, v127\n\t"
    "v_mfma_f32_16x16x32_bf16 v[188:191], v[120:123], v[12:15], v[188:191]\n\t"
    "buffer_load_dwordx4 v249, %[srdV], s64 offen offset:2048 nt lds\n\t"
    "v_fma_f32 v82, v82, s80, v247\n\t"
    "v_mfma_f32_16x16x32_bf16 v[192:195], v[120:123], v[104:107], v[192:195]\n\t"
    "v_exp_f32 v80, v80\n\t"
    "v_mfma_f32_16x16x32_bf16 v[196:199], v[120:123], v[28:31], v[196:199]\n\t"
    "v_fma_f32 v83, v83, s80, v247\n\t"
    "s_waitcnt lgkmcnt(5)\n\t"
    "v_mfma_f32_16x16x32_bf16 v[40:43], v[128:131], v[100:103], v[40:43]\n\t"
    "v_exp_f32 v81, v81\n\t"
    "v_mfma_f32_16x16x32_bf16 v[200:203], v[128:131], v[72:75], v[200:203]\n\t"
    "v_fma_f32 v132, v132, s80, v247\n\t"
    "v_cvt_pk_bf16_f32 v0, v76, v77\n\t"
    "v_mfma_f32_16x16x32_bf16 v[204:207], v[128:131], v[12:15], v[204:207]\n\t"
    "v_exp_f32 v82, v82\n\t"
    "v_mfma_f32_16x16x32_bf16 v[208:211], v[128:131], v[104:107], v[208:211]\n\t"
    "v_fma_f32 v133, v133, s80, v247\n\t"
    "v_cvt_pk_bf16_f32 v1, v78, v79\n\t"
    "v_mfma_f32_16x16x32_bf16 v[212:215], v[128:131], v[28:31], v[212:215]\n\t"
    "v_exp_f32 v83, v83\n\t"
    "s_waitcnt lgkmcnt(3)\n\t"
    "v_mfma_f32_16x16x32_bf16 v[44:47], v[140:143], v[100:103], v[44:47]\n\t"
    "v_fma_f32 v134, v134, s80, v247\n\t"
    "v_cvt_pk_bf16_f32 v2, v124, v125\n\t"
    "v_mfma_f32_16x16x32_bf16 v[216:219], v[140:143], v[72:75], v[216:219]\n\t"
    "buffer_load_dwordx4 v243, %[srdV], s64 offen offset:3072 nt lds\n\t"
    "v_mfma_f32_16x16x32_bf16 v[220:223], v[140:143], v[12:15], v[220:223]\n\t"
    "s_waitcnt vmcnt(12)\n\t"
    "ds_read_b64_tr_b16 v[16:17], v242 offset:12288\n\t"
    "ds_read_b64_tr_b16 v[18:19], v242 offset:14336\n\t"
    "v_mfma_f32_16x16x32_bf16 v[224:227], v[140:143], v[104:107], v[224:227]\n\t"
    "s_add_u32 m0, s65, 8192\n\t"
    "s_mov_b32 s64, 49152\n\t"
    "s_waitcnt lgkmcnt(3)\n\t"
    "buffer_load_dwordx4 v251, %[srdV], s64 offen offset:0 nt lds\n\t"
    "v_mfma_f32_16x16x32_bf16 v[228:231], v[140:143], v[28:31], v[228:231]\n\t"
    "ds_read_b64_tr_b16 v[76:77], v241 offset:12288\n\t"
    "ds_read_b64_tr_b16 v[78:79], v241 offset:14336\n\t"
    "v_mfma_f32_16x16x32_bf16 v[48:51], v[88:91], v[100:103], v[48:51]\n\t"
    "ds_read_b64_tr_b16 v[92:93], v240 offset:12288\n\t"
    "ds_read_b64_tr_b16 v[94:95], v240 offset:14336\n\t"
    "ds_read_b64_tr_b16 v[96:97], v236 offset:12288\n\t"
    "ds_read_b64_tr_b16 v[98:99], v236 offset:14336\n\t"
    "v_mfma_f32_16x16x32_bf16 v[56:59], v[88:91], v[72:75], v[56:59]\n\t"
    "v_exp_f32 v132, v132\n\t"
    "v_mfma_f32_16x16x32_bf16 v[60:63], v[88:91], v[12:15], v[60:63]\n\t"
    "v_fma_f32 v135, v135, s80, v247\n\t"
    "v_cvt_pk_bf16_f32 v3, v126, v127\n\t"
    "v_mfma_f32_16x16x32_bf16 v[64:67], v[88:91], v[104:107], v[64:67]\n\t"
    "v_exp_f32 v133, v133\n\t"
    "v_mfma_f32_16x16x32_bf16 v[68:71], v[88:91], v[28:31], v[68:71]\n\t"
    "v_fma_f32 v84, v84, s80, v246\n\t"
    "ds_write_b128 v235, v[0:3] offset:11264\n\t"
    "v_exp_f32 v134, v134\n\t"
    "v_fma_f32 v85, v85, s80, v246\n\t"
    "v_mfma_f32_16x16x32_bf16 v[4:7], v[36:39], v[0:3], v[4:7]\n\t"
    "v_exp_f32 v135, v135\n\t"
    "v_fma_f32 v86, v86, s80, v246\n\t"
    "v_exp_f32 v84, v84\n\t"
    "v_fma_f32 v87, v87, s80, v246\n\t"
    "v_exp_f32 v85, v85\n\t"
    "v_fma_f32 v136, v136, s80, v246\n\t"
    "v_cvt_pk_bf16_f32 v0, v80, v81\n\t"
    "v_exp_f32 v86, v86\n\t"
    "v_fma_f32 v137, v137, s80, v246\n\t"
    "v_cvt_pk_bf16_f32 v1, v82, v83\n\t"
    "v_exp_f32 v87, v87\n\t"
    "v_fma_f32 v138, v138, s80, v246\n\t"
    "v_cvt_pk_bf16_f32 v2, v132, v133\n\t"
    "v_exp_f32 v136, v136\n\t"
    "v_fma_f32 v139, v139, s80, v246\n\t"
    "v_cvt_pk_bf16_f32 v3, v134, v135\n\t"
    "v_exp_f32 v137, v137\n\t"
    "v_fma_f32 v144, v144, s80, v245\n\t"
    "ds_write_b128 v235, v[0:3] offset:12288\n\t"
    "v_exp_f32 v138, v138\n\t"
    "v_fma_f32 v145, v145, s80, v245\n\t"
    "v_mfma_f32_16x16x32_bf16 v[8:11], v[36:39], v[0:3], v[8:11]\n\t"
    "v_exp_f32 v139, v139\n\t"
    "v_fma_f32 v146, v146, s80, v245\n\t"
    "v_exp_f32 v144, v144\n\t"
    "v_fma_f32 v147, v147, s80, v245\n\t"
    "v_exp_f32 v145, v145\n\t"
    "v_fma_f32 v148, v148, s80, v245\n\t"
    "v_cvt_pk_bf16_f32 v0, v84, v85\n\t"
    "v_exp_f32 v146, v146\n\t"
    "v_fma_f32 v149, v149, s80, v245\n\t"
    "v_cvt_pk_bf16_f32 v1, v86, v87\n\t"
    "v_exp_f32 v147, v147\n\t"
    "v_fma_f32 v150, v150, s80, v245\n\t"
    "v_cvt_pk_bf16_f32 v2, v136, v137\n\t"
    "v_exp_f32 v148, v148\n\t"
    "v_fma_f32 v151, v151, s80, v245\n\t"
    "v_cvt_pk_bf16_f32 v3, v138, v139\n\t"
    "v_exp_f32 v149, v149\n\t"
    "ds_write_b128 v235, v[0:3] offset:13312\n\t"
    "v_exp_f32 v150, v150\n\t"
    "v_mfma_f32_16x16x32_bf16 v[20:23], v[36:39], v[0:3], v[20:23]\n\t"
    "v_exp_f32 v151, v151\n\t"
    "v_cvt_pk_bf16_f32 v0, v144, v145\n\t"
    "v_cvt_pk_bf16_f32 v1, v146, v147\n\t"
    "v_cvt_pk_bf16_f32 v2, v148, v149\n\t"
    "v_cvt_pk_bf16_f32 v3, v150, v151\n\t"
    "ds_write_b128 v235, v[0:3] offset:14336\n\t"
    "s_nop 0\n\t"
    "v_mfma_f32_16x16x32_bf16 v[24:27], v[36:39], v[0:3], v[24:27]\n\t"
    "s_waitcnt lgkmcnt(0)\n\t"
    "s_barrier\n\t"
    "ds_read_b128 v[0:3], v254 offset:10240\n\t"
    "ds_read_b128 v[12:15], v254 offset:11264\n\t"
    "ds_read_b128 v[28:31], v254 offset:12288\n\t"
    "ds_read_b128 v[72:75], v254 offset:13312\n\t"
    "ds_read_b128 v[80:83], v254 offset:14336\n\t"
    "s_waitcnt lgkmcnt(4)\n\t"
    "v_mfma_f32_16x16x32_bf16 v[180:183], v[16:19], v[0:3], v[180:183]\n\t"
    "buffer_load_dwordx4 v250, %[srdV], s64 offen offset:1024 nt lds\n\t"
    "ds_read_b128 v[84:87], v254 offset:30720\n\t"
    "s_waitcnt lgkmcnt(4)\n\t"
    "v_mfma_f32_16x16x32_bf16 v[184:187], v[16:19], v[12:15], v[184:187]\n\t"
    "buffer_load_dwordx4 v249, %[srdV], s64 offen offset:2048 nt lds\n\t"
    "v_fma_f32 v32, v32, s80, v255\n\t"
    "s_waitcnt lgkmcnt(3)\n\t"
    "v_mfma_f32_16x16x32_bf16 v[188:191], v[16:19], v[28:31], v[188:191]\n\t"
    "buffer_load_dwordx4 v243, %[srdV], s64 offen offset:3072 nt lds\n\t"
    "ds_read_b128 v[88:91], v254 offset:31744\n\t"
    "s_waitcnt lgkmcnt(3)\n\t"
    "v_mfma_f32_16x16x32_bf16 v[192:195], v[16:19], v[72:75], v[192:195]\n\t"
    "v_fma_f32 v33, v33, s80, v255\n\t"
    "v_fma_f32 v34, v34, s80, v255\n\t"
    "s_waitcnt lgkmcnt(2)\n\t"
    "v_mfma_f32_16x16x32_bf16 v[196:199], v[16:19], v[80:83], v[196:199]\n\t"
    "ds_read_b128 v[16:19], v254 offset:32768\n\t"
    "v_mfma_f32_16x16x32_bf16 v[40:43], v[76:79], v[0:3], v[40:43]\n\t"
    "v_exp_f32 v32, v32\n\t"
    "v_mfma_f32_16x16x32_bf16 v[200:203], v[76:79], v[12:15], v[200:203]\n\t"
    "ds_read_b128 v[100:103], v254 offset:33792\n\t"
    "v_fma_f32 v35, v35, s80, v255\n\t"
    "v_mfma_f32_16x16x32_bf16 v[204:207], v[76:79], v[28:31], v[204:207]\n\t"
    "s_add_u32 m0, s65, 12288\n\t"
    "s_mov_b32 s64, 114688\n\t"
    "buffer_load_dwordx4 v251, %[srdV], s64 offen offset:0 nt lds\n\t"
    "v_mfma_f32_16x16x32_bf16 v[208:211], v[76:79], v[72:75], v[208:211]\n\t"
    "ds_read_b128 v[104:107], v254 offset:34816\n\t"
    "v_mfma_f32_16x16x32_bf16 v[212:215], v[76:79], v[80:83], v[212:215]\n\t"
    "v_exp_f32 v33, v33\n\t"
    "v_mfma_f32_16x16x32_bf16 v[44:47], v[92:95], v[0:3], v[44:47]\n\t"
    "v_fma_f32 v152, v152, s80, v255\n\t"
    "v_mfma_f32_16x16x32_bf16 v[216:219], v[92:95], v[12:15], v[216:219]\n\t"
    "v_exp_f32 v34, v34\n\t"
    "v_mfma_f32_16x16x32_bf16 v[220:223], v[92:95], v[28:31], v[220:223]\n\t"
    "s_waitcnt vmcnt(13)\n\t"
    "ds_read_b64_tr_b16 v[76:77], v242 offset:16384\n\t"
    "ds_read_b64_tr_b16 v[78:79], v242 offset:18432\n\t"
    "v_mfma_f32_16x16x32_bf16 v[224:227], v[92:95], v[72:75], v[224:227]\n\t"
    "ds_read_b64_tr_b16 v[120:121], v241 offset:16384\n\t"
    "ds_read_b64_tr_b16 v[122:123], v241 offset:18432\n\t"
    "v_mfma_f32_16x16x32_bf16 v[228:231], v[92:95], v[80:83], v[228:231]\n\t"
    "ds_read_b64_tr_b16 v[92:93], v240 offset:16384\n\t"
    "ds_read_b64_tr_b16 v[94:95], v240 offset:18432\n\t"
    "v_mfma_f32_16x16x32_bf16 v[48:51], v[96:99], v[0:3], v[48:51]\n\t"
    "buffer_load_dwordx4 v250, %[srdV], s64 offen offset:1024 nt lds\n\t"
    "ds_read_b64_tr_b16 v[0:1], v236 offset:16384\n\t"
    "ds_read_b64_tr_b16 v[2:3], v236 offset:18432\n\t"
    "v_mfma_f32_16x16x32_bf16 v[56:59], v[96:99], v[12:15], v[56:59]\n\t"
    "v_fma_f32 v153, v153, s80, v255\n\t"
    "v_mfma_f32_16x16x32_bf16 v[60:63], v[96:99], v[28:31], v[60:63]\n\t"
    "v_exp_f32 v35, v35\n\t"
    "v_mfma_f32_16x16x32_bf16 v[64:67], v[96:99], v[72:75], v[64:67]\n\t"
    "v_fma_f32 v154, v154, s80, v255\n\t"
    "v_mfma_f32_16x16x32_bf16 v[68:71], v[96:99], v[80:83], v[68:71]\n\t"
    "v_exp_f32 v152, v152\n\t"
    "s_waitcnt lgkmcnt(6)\n\t"
    "v_mfma_f32_16x16x32_bf16 v[180:183], v[76:79], v[84:87], v[180:183]\n\t"
    "ds_read_b128 v[12:15], v253 offset:10240\n\t"
    "v_fma_f32 v155, v155, s80, v255\n\t"
    "v_mfma_f32_16x16x32_bf16 v[184:187], v[76:79], v[88:91], v[184:187]\n\t"
    "v_exp_f32 v153, v153\n\t"
    "v_mfma_f32_16x16x32_bf16 v[188:191], v[76:79], v[16:19], v[188:191]\n\t"
    "ds_read_b128 v[28:31], v253 offset:11264\n\t"
    "v_fma_f32 v108, v108, s80, v248\n\t"
    "v_mfma_f32_16x16x32_bf16 v[192:195], v[76:79], v[100:103], v[192:195]\n\t"
    "buffer_load_dwordx4 v249, %[srdV], s64 offen offset:2048 nt lds\n\t"
    "v_mfma_f32_16x16x32_bf16 v[196:199], v[76:79], v[104:107], v[196:199]\n\t"
    "ds_read_b128 v[72:75], v253 offset:12288\n\t"
    "s_waitcnt lgkmcnt(7)\n\t"
    "v_mfma_f32_16x16x32_bf16 v[40:43], v[120:123], v[84:87], v[40:43]\n\t"
    "v_exp_f32 v154, v154\n\t"
    "v_mfma_f32_16x16x32_bf16 v[200:203], v[120:123], v[88:91], v[200:203]\n\t"
    "ds_read_b128 v[76:79], v253 offset:13312\n\t"
    "v_fma_f32 v109, v109, s80, v248\n\t"
    "v_mfma_f32_16x16x32_bf16 v[204:207], v[120:123], v[16:19], v[204:207]\n\t"
    "v_exp_f32 v155, v155\n\t"
    "v_mfma_f32_16x16x32_bf16 v[208:211], v[120:123], v[100:103], v[208:211]\n\t"
    "ds_read_b128 v[80:83], v253 offset:14336\n\t"
    "v_fma_f32 v110, v110, s80, v248\n\t"
    "v_mfma_f32_16x16x32_bf16 v[212:215], v[120:123], v[104:107], v[212:215]\n\t"
    "v_exp_f32 v108, v108\n\t"
    "s_waitcnt lgkmcnt(7)\n\t"
    "v_mfma_f32_16x16x32_bf16 v[44:47], v[92:95], v[84:87], v[44:47]\n\t"
    "v_fma_f32 v111, v111, s80, v248\n\t"
    "v_mfma_f32_16x16x32_bf16 v[216:219], v[92:95], v[88:91], v[216:219]\n\t"
    "v_exp_f32 v109, v109\n\t"
    "v_mfma_f32_16x16x32_bf16 v[220:223], v[92:95], v[16:19], v[220:223]\n\t"
    "buffer_load_dwordx4 v243, %[srdV], s64 offen offset:3072 nt lds\n\t"
    "s_waitcnt vmcnt(12)\n\t"
    "ds_read_b64_tr_b16 v[96:97], v242 offset:0\n\t"
    "v_mfma_f32_16x16x32_bf16 v[224:227], v[92:95], v[100:103], v[224:227]\n\t"
    "s_add_u32 m0, s65, 16384\n\t"
    "s_mov_b32 s64, 180224\n\t"
    "s_waitcnt lgkmcnt(6)\n\t"
    "buffer_load_dwordx4 v251, %[srdV], s64 offen offset:0 nt lds\n\t"
    "ds_read_b64_tr_b16 v[98:99], v242 offset:2048\n\t"
    "v_mfma_f32_16x16x32_bf16 v[228:231], v[92:95], v[104:107], v[228:231]\n\t"
    "ds_read_b64_tr_b16 v[92:93], v241 offset:0\n\t"
    "ds_read_b64_tr_b16 v[94:95], v241 offset:2048\n\t"
    "v_mfma_f32_16x16x32_bf16 v[48:51], v[0:3], v[84:87], v[48:51]\n\t"
    "ds_read_b64_tr_b16 v[84:85], v240 offset:0\n\t"
    "ds_read_b64_tr_b16 v[86:87], v240 offset:2048\n\t"
    "ds_read_b64_tr_b16 v[120:121], v236 offset:0\n\t"
    "ds_read_b64_tr_b16 v[122:123], v236 offset:2048\n\t"
    "v_mfma_f32_16x16x32_bf16 v[56:59], v[0:3], v[88:91], v[56:59]\n\t"
    "v_fma_f32 v156, v156, s80, v248\n\t"
    "v_cvt_pk_bf16_f32 v88, v32, v33\n\t"
    "v_mfma_f32_16x16x32_bf16 v[60:63], v[0:3], v[16:19], v[60:63]\n\t"
    "v_exp_f32 v110, v110\n\t"
    "v_mfma_f32_16x16x32_bf16 v[64:67], v[0:3], v[100:103], v[64:67]\n\t"
    "v_fma_f32 v157, v157, s80, v248\n\t"
    "v_cvt_pk_bf16_f32 v89, v34, v35\n\t"
    "v_mfma_f32_16x16x32_bf16 v[68:71], v[0:3], v[104:107], v[68:71]\n\t"
    "v_exp_f32 v111, v111\n\t"
    "s_waitcnt lgkmcnt(6)\n\t"
    "v_mfma_f32_16x16x32_bf16 v[180:183], v[96:99], v[12:15], v[180:183]\n\t"
    "buffer_load_dwordx4 v250, %[srdV], s64 offen offset:1024 nt lds\n\t"
    "ds_read_b128 v[0:3], v253 offset:30720\n\t"
    "v_mfma_f32_16x16x32_bf16 v[184:187], v[96:99], v[28:31], v[184:187]\n\t"
    "v_fma_f32 v158, v158, s80, v248\n\t"
    "v_cvt_pk_bf16_f32 v90, v152, v153\n\t"
    "v_mfma_f32_16x16x32_bf16 v[188:191], v[96:99], v[72:75], v[188:191]\n\t"
    "ds_read_b128 v[16:19], v253 offset:31744\n\t"
    "v_mfma_f32_16x16x32_bf16 v[192:195], v[96:99], v[76:79], v[192:195]\n\t"
    "v_exp_f32 v156, v156\n\t"
    "v_mfma_f32_16x16x32_bf16 v[196:199], v[96:99], v[80:83], v[196:199]\n\t"
    "ds_read_b128 v[32:35], v253 offset:32768\n\t"
    "v_fma_f32 v159, v159, s80, v248\n\t"
    "s_waitcnt lgkmcnt(7)\n\t"
    "v_mfma_f32_16x16x32_bf16 v[40:43], v[92:95], v[12:15], v[40:43]\n\t"
    "v_cvt_pk_bf16_f32 v91, v154, v155\n\t"
    "v_mfma_f32_16x16x32_bf16 v[200:203], v[92:95], v[28:31], v[200:203]\n\t"
    "ds_read_b128 v[96:99], v253 offset:33792\n\t"
    "v_mfma_f32_16x16x32_bf16 v[204:207], v[92:95], v[72:75], v[204:207]\n\t"
    "v_exp_f32 v157, v157\n\t"
    "v_mfma_f32_16x16x32_bf16 v[208:211], v[92:95], v[76:79], v[208:211]\n\t"
    "ds_read_b128 v[100:103], v253 offset:34816\n\t"
    "v_fma_f32 v112, v112, s80, v247\n\t"
    "v_mfma_f32_16x16x32_bf16 v[212:215], v[92:95], v[80:83], v[212:215]\n\t"
    "buffer_load_dwordx4 v249, %[srdV], s64 offen offset:2048 nt lds\n\t"
    "ds_write_b128 v235, v[88:91] offset:15360\n\t"
    "s_waitcnt lgkmcnt(8)\n\t"
    "v_mfma_f32_16x16x32_bf16 v[44:47], v[84:87], v[12:15], v[44:47]\n\t"
    "v_exp_f32 v158, v158\n\t"
    "v_mfma_f32_16x16x32_bf16 v[216:219], v[84:87], v[28:31], v[216:219]\n\t"
    "v_fma_f32 v113, v113, s80, v247\n\t"
    "v_mfma_f32_16x16x32_bf16 v[220:223], v[84:87], v[72:75], v[220:223]\n\t"
    "s_waitcnt vmcnt(11)\n\t"
    "ds_read_b64_tr_b16 v[92:93], v242 offset:4096\n\t"
    "buffer_load_dwordx4 v243, %[srdV], s64 offen offset:3072 nt lds\n\t"
    "v_mfma_f32_16x16x32_bf16 v[224:227], v[84:87], v[76:79], v[224:227]\n\t"
    "s_add_u32 m0, s65, 0\n\t"
    "s_mov_b32 s64, 245760\n\t"
    "s_waitcnt lgkmcnt(7)\n\t"
    "buffer_load_dwordx4 v251, %[srdV], s64 offen offset:0 nt lds\n\t"
    "ds_read_b64_tr_b16 v[94:95], v242 offset:6144\n\t"
    "v_mfma_f32_16x16x32_bf16 v[228:231], v[84:87], v[80:83], v[228:231]\n\t"
    "ds_read_b64_tr_b16 v[84:85], v241 offset:4096\n\t"
    "ds_read_b64_tr_b16 v[86:87], v241 offset:6144\n\t"
    "v_mfma_f32_16x16x32_bf16 v[48:51], v[120:123], v[12:15], v[48:51]\n\t"
    "ds_read_b64_tr_b16 v[12:13], v240 offset:4096\n\t"
    "ds_read_b64_tr_b16 v[14:15], v240 offset:6144\n\t"
    "ds_read_b64_tr_b16 v[104:105], v236 offset:4096\n\t"
    "ds_read_b64_tr_b16 v[106:107], v236 offset:6144\n\t"
    "v_mfma_f32_16x16x32_bf16 v[56:59], v[120:123], v[28:31], v[56:59]\n\t"
    "v_mfma_f32_16x16x32_bf16 v[52:55], v[36:39], v[88:91], v[52:55]\n\t"
    "v_mfma_f32_16x16x32_bf16 v[60:63], v[120:123], v[72:75], v[60:63]\n\t"
    "v_exp_f32 v159, v159\n\t"
    "v_mfma_f32_16x16x32_bf16 v[64:67], v[120:123], v[76:79], v[64:67]\n\t"
    "v_fma_f32 v114, v114, s80, v247\n\t"
    "v_mfma_f32_16x16x32_bf16 v[68:71], v[120:123], v[80:83], v[68:71]\n\t"
    "v_exp_f32 v112, v112\n\t"
    "s_waitcnt lgkmcnt(6)\n\t"
    "v_mfma_f32_16x16x32_bf16 v[180:183], v[92:95], v[0:3], v[180:183]\n\t"
    "buffer_load_dwordx4 v250, %[srdV], s64 offen offset:1024 nt lds\n\t"
    "v_fma_f32 v115, v115, s80, v247\n\t"
    "v_mfma_f32_16x16x32_bf16 v[184:187], v[92:95], v[16:19], v[184:187]\n\t"
    "v_exp_f32 v113, v113\n\t"
    "v_mfma_f32_16x16x32_bf16 v[188:191], v[92:95], v[32:35], v[188:191]\n\t"
    "v_fma_f32 v164, v164, s80, v247\n\t"
    "v_cvt_pk_bf16_f32 v28, v108, v109\n\t"
    "v_mfma_f32_16x16x32_bf16 v[192:195], v[92:95], v[96:99], v[192:195]\n\t"
    "v_exp_f32 v114, v114\n\t"
    "v_mfma_f32_16x16x32_bf16 v[196:199], v[92:95], v[100:103], v[196:199]\n\t"
    "v_fma_f32 v165, v165, s80, v247\n\t"
    "v_cvt_pk_bf16_f32 v29, v110, v111\n\t"
    "s_waitcnt lgkmcnt(4)\n\t"
    "v_mfma_f32_16x16x32_bf16 v[40:43], v[84:87], v[0:3], v[40:43]\n\t"
    "v_exp_f32 v115, v115\n\t"
    "v_mfma_f32_16x16x32_bf16 v[200:203], v[84:87], v[16:19], v[200:203]\n\t"
    "v_fma_f32 v166, v166, s80, v247\n\t"
    "v_cvt_pk_bf16_f32 v30, v156, v157\n\t"
    "v_mfma_f32_16x16x32_bf16 v[204:207], v[84:87], v[32:35], v[204:207]\n\t"
    "v_exp_f32 v164, v164\n\t"
    "v_mfma_f32_16x16x32_bf16 v[208:211], v[84:87], v[96:99], v[208:211]\n\t"
    "v_fma_f32 v167, v167, s80, v247\n\t"
    "v_cvt_pk_bf16_f32 v31, v158, v159\n\t"
    "v_mfma_f32_16x16x32_bf16 v[212:215], v[84:87], v[100:103], v[212:215]\n\t"
    "v_exp_f32 v165, v165\n\t"
    "s_waitcnt lgkmcnt(2)\n\t"
    "v_mfma_f32_16x16x32_bf16 v[44:47], v[12:15], v[0:3], v[44:47]\n\t"
    "v_fma_f32 v116, v116, s80, v246\n\t"
    "ds_write_b128 v235, v[28:31] offset:16384\n\t"
    "v_mfma_f32_16x16x32_bf16 v[216:219], v[12:15], v[16:19], v[216:219]\n\t"
    "buffer_load_dwordx4 v249, %[srdV], s64 offen offset:2048 nt lds\n\t"
    "v_mfma_f32_16x16x32_bf16 v[220:223], v[12:15], v[32:35], v[220:223]\n\t"
    "s_waitcnt vmcnt(11)\n\t"
    "ds_read_b64_tr_b16 v[72:73], v242 offset:8192\n\t"
    "buffer_load_dwordx4 v243, %[srdV], s64 offen offset:3072 nt lds\n\t"
    "v_mfma_f32_16x16x32_bf16 v[224:227], v[12:15], v[96:99], v[224:227]\n\t"
    "v_mbcnt_lo_u32_b32 v255, -1, 0\n\t"
    "v_mbcnt_hi_u32_b32 v255, -1, v255\n\t"
    "v_lshrrev_b32 v251, 3, v255\n\t"
    "v_and_b32 v250, 6, v251\n\t"
    "v_xor_b32 v250, v250, v255\n\t"
    "v_and_b32 v250, 7, v250\n\t"
    "v_mul_u32_u24 v255, 896, v251\n\t"
    "v_lshl_add_u32 v251, v250, 4, v255\n\t"
    "v_add_u32 v255, 6144, v251\n\t"
    "v_add_u32 v250, 12288, v251\n\t"
    "v_add_u32 v249, 18432, v251\n\t"
    "s_add_u32 m0, s65, 4096\n\t"
    "s_add_u32 s64, s81, 0\n\t"
    "s_waitcnt lgkmcnt(2)\n\t"
    "buffer_load_dwordx4 v251, %[srdWo], s64 offen offset:0 lds\n\t"
    "v_mfma_f32_16x16x32_bf16 v[228:231], v[12:15], v[100:103], v[228:231]\n\t"
    "ds_read_b64_tr_b16 v[74:75], v242 offset:10240\n\t"
    "ds_read_b64_tr_b16 v[12:13], v241 offset:8192\n\t"
    "ds_read_b64_tr_b16 v[14:15], v241 offset:10240\n\t"
    "v_mfma_f32_16x16x32_bf16 v[48:51], v[104:107], v[0:3], v[48:51]\n\t"
    "ds_read_b64_tr_b16 v[0:1], v240 offset:8192\n\t"
    "ds_read_b64_tr_b16 v[2:3], v240 offset:10240\n\t"
    "ds_read_b64_tr_b16 v[76:77], v236 offset:8192\n\t"
    "ds_read_b64_tr_b16 v[78:79], v236 offset:10240\n\t"
    "v_mfma_f32_16x16x32_bf16 v[56:59], v[104:107], v[16:19], v[56:59]\n\t"
    "v_exp_f32 v166, v166\n\t"
    "v_mfma_f32_16x16x32_bf16 v[60:63], v[104:107], v[32:35], v[60:63]\n\t"
    "buffer_load_dwordx4 v255, %[srdWo], s64 offen offset:1024 lds\n\t"
    "v_fma_f32 v117, v117, s80, v246\n\t"
    "v_mfma_f32_16x16x32_bf16 v[64:67], v[104:107], v[96:99], v[64:67]\n\t"
    "v_mfma_f32_16x16x32_bf16 v[4:7], v[36:39], v[28:31], v[4:7]\n\t"
    "v_mfma_f32_16x16x32_bf16 v[68:71], v[104:107], v[100:103], v[68:71]\n\t"
    "v_exp_f32 v167, v167\n\t"
    "v_fma_f32 v118, v118, s80, v246\n\t"
    "v_exp_f32 v116, v116\n\t"
    "v_fma_f32 v119, v119, s80, v246\n\t"
    "v_exp_f32 v117, v117\n\t"
    "v_fma_f32 v168, v168, s80, v246\n\t"
    "v_cvt_pk_bf16_f32 v16, v112, v113\n\t"
    "v_exp_f32 v118, v118\n\t"
    "v_fma_f32 v169, v169, s80, v246\n\t"
    "v_cvt_pk_bf16_f32 v17, v114, v115\n\t"
    "v_exp_f32 v119, v119\n\t"
    "v_fma_f32 v170, v170, s80, v246\n\t"
    "v_cvt_pk_bf16_f32 v18, v164, v165\n\t"
    "v_exp_f32 v168, v168\n\t"
    "v_fma_f32 v171, v171, s80, v246\n\t"
    "v_cvt_pk_bf16_f32 v19, v166, v167\n\t"
    "v_exp_f32 v169, v169\n\t"
    "v_fma_f32 v160, v160, s80, v245\n\t"
    "ds_write_b128 v235, v[16:19] offset:17408\n\t"
    "v_exp_f32 v170, v170\n\t"
    "v_fma_f32 v161, v161, s80, v245\n\t"
    "v_mfma_f32_16x16x32_bf16 v[8:11], v[36:39], v[16:19], v[8:11]\n\t"
    "v_exp_f32 v171, v171\n\t"
    "v_fma_f32 v162, v162, s80, v245\n\t"
    "v_exp_f32 v160, v160\n\t"
    "v_fma_f32 v163, v163, s80, v245\n\t"
    "v_exp_f32 v161, v161\n\t"
    "v_fma_f32 v172, v172, s80, v245\n\t"
    "v_cvt_pk_bf16_f32 v16, v116, v117\n\t"
    "v_exp_f32 v162, v162\n\t"
    "v_fma_f32 v173, v173, s80, v245\n\t"
    "v_cvt_pk_bf16_f32 v17, v118, v119\n\t"
    "v_exp_f32 v163, v163\n\t"
    "v_fma_f32 v174, v174, s80, v245\n\t"
    "v_cvt_pk_bf16_f32 v18, v168, v169\n\t"
    "v_exp_f32 v172, v172\n\t"
    "v_fma_f32 v175, v175, s80, v245\n\t"
    "v_cvt_pk_bf16_f32 v19, v170, v171\n\t"
    "v_exp_f32 v173, v173\n\t"
    "ds_write_b128 v235, v[16:19] offset:18432\n\t"
    "v_exp_f32 v174, v174\n\t"
    "v_mfma_f32_16x16x32_bf16 v[20:23], v[36:39], v[16:19], v[20:23]\n\t"
    "v_exp_f32 v175, v175\n\t"
    "v_cvt_pk_bf16_f32 v16, v160, v161\n\t"
    "v_cvt_pk_bf16_f32 v17, v162, v163\n\t"
    "v_cvt_pk_bf16_f32 v18, v172, v173\n\t"
    "v_cvt_pk_bf16_f32 v19, v174, v175\n\t"
    "ds_write_b128 v235, v[16:19] offset:19456\n\t"
    "s_nop 0\n\t"
    "v_mfma_f32_16x16x32_bf16 v[24:27], v[36:39], v[16:19], v[24:27]\n\t"
    "s_waitcnt lgkmcnt(0)\n\t"
    "s_barrier\n\t"
    "ds_read_b128 v[16:19], v254 offset:15360\n\t"
    "ds_read_b128 v[28:31], v254 offset:16384\n\t"
    "ds_read_b128 v[32:35], v254 offset:17408\n\t"
    "ds_read_b128 v[36:39], v254 offset:18432\n\t"
    "ds_read_b128 v[80:83], v254 offset:19456\n\t"
    "s_waitcnt lgkmcnt(4)\n\t"
    "v_mfma_f32_16x16x32_bf16 v[180:183], v[72:75], v[16:19], v[180:183]\n\t"
    "buffer_load_dwordx4 v250, %[srdWo], s64 offen offset:2048 lds\n\t"
    "ds_read_b128 v[84:87], v254 offset:35840\n\t"
    "s_waitcnt lgkmcnt(4)\n\t"
    "v_mfma_f32_16x16x32_bf16 v[184:187], v[72:75], v[28:31], v[184:187]\n\t"
    "buffer_load_dwordx4 v249, %[srdWo], s64 offen offset:3072 lds\n\t"
    "s_waitcnt lgkmcnt(3)\n\t"
    "v_mfma_f32_16x16x32_bf16 v[188:191], v[72:75], v[32:35], v[188:191]\n\t"
    "ds_read_b128 v[88:91], v254 offset:36864\n\t"
    "s_waitcnt lgkmcnt(3)\n\t"
    "v_mfma_f32_16x16x32_bf16 v[192:195], v[72:75], v[36:39], v[192:195]\n\t"
    "s_waitcnt lgkmcnt(2)\n\t"
    "v_mfma_f32_16x16x32_bf16 v[196:199], v[72:75], v[80:83], v[196:199]\n\t"
    "ds_read_b128 v[72:75], v254 offset:37888\n\t"
    "v_mfma_f32_16x16x32_bf16 v[40:43], v[12:15], v[16:19], v[40:43]\n\t"
    "v_mfma_f32_16x16x32_bf16 v[200:203], v[12:15], v[28:31], v[200:203]\n\t"
    "s_add_u32 m0, s65, 8192\n\t"
    "s_add_u32 s64, s81, 28672\n\t"
    "buffer_load_dwordx4 v251, %[srdWo], s64 offen offset:0 lds\n\t"
    "v_mfma_f32_16x16x32_bf16 v[204:207], v[12:15], v[32:35], v[204:207]\n\t"
    "ds_read_b128 v[92:95], v254 offset:38912\n\t"
    "v_mfma_f32_16x16x32_bf16 v[208:211], v[12:15], v[36:39], v[208:211]\n\t"
    "v_mfma_f32_16x16x32_bf16 v[212:215], v[12:15], v[80:83], v[212:215]\n\t"
    "ds_read_b128 v[12:15], v254 offset:39936\n\t"
    "v_mfma_f32_16x16x32_bf16 v[44:47], v[0:3], v[16:19], v[44:47]\n\t"
    "v_mfma_f32_16x16x32_bf16 v[216:219], v[0:3], v[28:31], v[216:219]\n\t"
    "s_waitcnt vmcnt(13)\n\t"
    "ds_read_b64_tr_b16 v[96:97], v242 offset:12288\n\t"
    "ds_read_b64_tr_b16 v[98:99], v242 offset:14336\n\t"
    "v_mfma_f32_16x16x32_bf16 v[220:223], v[0:3], v[32:35], v[220:223]\n\t"
    "ds_read_b64_tr_b16 v[100:101], v241 offset:12288\n\t"
    "ds_read_b64_tr_b16 v[102:103], v241 offset:14336\n\t"
    "v_mfma_f32_16x16x32_bf16 v[224:227], v[0:3], v[36:39], v[224:227]\n\t"
    "ds_read_b64_tr_b16 v[104:105], v240 offset:12288\n\t"
    "ds_read_b64_tr_b16 v[106:107], v240 offset:14336\n\t"
    "v_mfma_f32_16x16x32_bf16 v[228:231], v[0:3], v[80:83], v[228:231]\n\t"
    "buffer_load_dwordx4 v255, %[srdWo], s64 offen offset:1024 lds\n\t"
    "ds_read_b64_tr_b16 v[0:1], v236 offset:12288\n\t"
    "v_mfma_f32_16x16x32_bf16 v[48:51], v[76:79], v[16:19], v[48:51]\n\t"
    "ds_read_b64_tr_b16 v[2:3], v236 offset:14336\n\t"
    "v_mfma_f32_16x16x32_bf16 v[56:59], v[76:79], v[28:31], v[56:59]\n\t"
    "v_mfma_f32_16x16x32_bf16 v[60:63], v[76:79], v[32:35], v[60:63]\n\t"
    "v_mfma_f32_16x16x32_bf16 v[64:67], v[76:79], v[36:39], v[64:67]\n\t"
    "v_mfma_f32_16x16x32_bf16 v[68:71], v[76:79], v[80:83], v[68:71]\n\t"
    "s_waitcnt lgkmcnt(6)\n\t"
    "v_mfma_f32_16x16x32_bf16 v[180:183], v[96:99], v[84:87], v[180:183]\n\t"
    "ds_read_b128 v[16:19], v253 offset:15360\n\t"
    "v_mbcnt_lo_u32_b32 v248, -1, 0\n\t"
    "v_mfma_f32_16x16x32_bf16 v[184:187], v[96:99], v[88:91], v[184:187]\n\t"
    "v_mbcnt_hi_u32_b32 v248, -1, v248\n\t"
    "v_lshlrev_b32 v247, 4, v248\n\t"
    "v_mfma_f32_16x16x32_bf16 v[188:191], v[96:99], v[72:75], v[188:191]\n\t"
    "buffer_load_dwordx4 v250, %[srdWo], s64 offen offset:2048 lds\n\t"
    "ds_read_b128 v[28:31], v253 offset:16384\n\t"
    "v_mfma_f32_16x16x32_bf16 v[192:195], v[96:99], v[92:95], v[192:195]\n\t"
    "v_lshlrev_b32 v246, 3, v248\n\t"
    "v_mfma_f32_16x16x32_bf16 v[196:199], v[96:99], v[12:15], v[196:199]\n\t"
    "ds_read_b128 v[32:35], v253 offset:17408\n\t"
    "s_waitcnt lgkmcnt(7)\n\t"
    "v_mfma_f32_16x16x32_bf16 v[40:43], v[100:103], v[84:87], v[40:43]\n\t"
    "v_mfma_f32_16x16x32_bf16 v[200:203], v[100:103], v[88:91], v[200:203]\n\t"
    "ds_read_b128 v[36:39], v253 offset:18432\n\t"
    "v_mfma_f32_16x16x32_bf16 v[204:207], v[100:103], v[72:75], v[204:207]\n\t"
    "v_mfma_f32_16x16x32_bf16 v[208:211], v[100:103], v[92:95], v[208:211]\n\t"
    "ds_read_b128 v[76:79], v253 offset:19456\n\t"
    "v_mfma_f32_16x16x32_bf16 v[212:215], v[100:103], v[12:15], v[212:215]\n\t"
    "s_waitcnt lgkmcnt(7)\n\t"
    "v_mfma_f32_16x16x32_bf16 v[44:47], v[104:107], v[84:87], v[44:47]\n\t"
    "v_mfma_f32_16x16x32_bf16 v[216:219], v[104:107], v[88:91], v[216:219]\n\t"
    "buffer_load_dwordx4 v249, %[srdWo], s64 offen offset:3072 lds\n\t"
    "v_mfma_f32_16x16x32_bf16 v[220:223], v[104:107], v[72:75], v[220:223]\n\t"
    "s_waitcnt vmcnt(12)\n\t"
    "ds_read_b64_tr_b16 v[80:81], v242 offset:16384\n\t"
    "ds_read_b64_tr_b16 v[82:83], v242 offset:18432\n\t"
    "v_mfma_f32_16x16x32_bf16 v[224:227], v[104:107], v[92:95], v[224:227]\n\t"
    "s_add_u32 m0, s65, 12288\n\t"
    "s_add_u32 s64, s81, 57344\n\t"
    "s_waitcnt lgkmcnt(7)\n\t"
    "buffer_load_dwordx4 v251, %[srdWo], s64 offen offset:0 lds\n\t"
    "v_mfma_f32_16x16x32_bf16 v[228:231], v[104:107], v[12:15], v[228:231]\n\t"
    "ds_read_b64_tr_b16 v[96:97], v241 offset:16384\n\t"
    "ds_read_b64_tr_b16 v[98:99], v241 offset:18432\n\t"
    "v_mfma_f32_16x16x32_bf16 v[48:51], v[0:3], v[84:87], v[48:51]\n\t"
    "ds_read_b64_tr_b16 v[84:85], v240 offset:16384\n\t"
    "ds_read_b64_tr_b16 v[86:87], v240 offset:18432\n\t"
    "ds_read_b64_tr_b16 v[100:101], v236 offset:16384\n\t"
    "ds_read_b64_tr_b16 v[102:103], v236 offset:18432\n\t"
    "v_mfma_f32_16x16x32_bf16 v[56:59], v[0:3], v[88:91], v[56:59]\n\t"
    "v_mfma_f32_16x16x32_bf16 v[60:63], v[0:3], v[72:75], v[60:63]\n\t"
    "v_mfma_f32_16x16x32_bf16 v[64:67], v[0:3], v[92:95], v[64:67]\n\t"
    "v_mfma_f32_16x16x32_bf16 v[68:71], v[0:3], v[12:15], v[68:71]\n\t"
    "s_waitcnt lgkmcnt(6)\n\t"
    "v_mfma_f32_16x16x32_bf16 v[180:183], v[80:83], v[16:19], v[180:183]\n\t"
    "buffer_load_dwordx4 v255, %[srdWo], s64 offen offset:1024 lds\n\t"
    "ds_read_b128 v[0:3], v253 offset:35840\n\t"
    "v_mfma_f32_16x16x32_bf16 v[184:187], v[80:83], v[28:31], v[184:187]\n\t"
    "v_mfma_f32_16x16x32_bf16 v[188:191], v[80:83], v[32:35], v[188:191]\n\t"
    "ds_read_b128 v[12:15], v253 offset:36864\n\t"
    "v_mfma_f32_16x16x32_bf16 v[192:195], v[80:83], v[36:39], v[192:195]\n\t"
    "v_mfma_f32_16x16x32_bf16 v[196:199], v[80:83], v[76:79], v[196:199]\n\t"
    "ds_read_b128 v[72:75], v253 offset:37888\n\t"
    "s_waitcnt lgkmcnt(7)\n\t"
    "v_mfma_f32_16x16x32_bf16 v[40:43], v[96:99], v[16:19], v[40:43]\n\t"
    "v_mfma_f32_16x16x32_bf16 v[200:203], v[96:99], v[28:31], v[200:203]\n\t"
    "ds_read_b128 v[80:83], v253 offset:38912\n\t"
    "v_mfma_f32_16x16x32_bf16 v[204:207], v[96:99], v[32:35], v[204:207]\n\t"
    "v_mfma_f32_16x16x32_bf16 v[208:211], v[96:99], v[36:39], v[208:211]\n\t"
    "ds_read_b128 v[88:91], v253 offset:39936\n\t"
    "v_mfma_f32_16x16x32_bf16 v[212:215], v[96:99], v[76:79], v[212:215]\n\t"
    "buffer_load_dwordx4 v250, %[srdWo], s64 offen offset:2048 lds\n\t"
    "s_waitcnt lgkmcnt(7)\n\t"
    "v_mfma_f32_16x16x32_bf16 v[44:47], v[84:87], v[16:19], v[44:47]\n\t"
    "v_mfma_f32_16x16x32_bf16 v[216:219], v[84:87], v[28:31], v[216:219]\n\t"
    "v_mfma_f32_16x16x32_bf16 v[220:223], v[84:87], v[32:35], v[220:223]\n\t"
    "s_waitcnt vmcnt(11)\n\t"
    "ds_read_b64_tr_b16 v[92:93], v242 offset:0\n\t"
    "buffer_load_dwordx4 v249, %[srdWo], s64 offen offset:3072 lds\n\t"
    "v_mfma_f32_16x16x32_bf16 v[224:227], v[84:87], v[36:39], v[224:227]\n\t"
    "s_add_u32 m0, s65, 16384\n\t"
    "s_add_u32 s64, s81, 86016\n\t"
    "s_waitcnt lgkmcnt(6)\n\t"
    "buffer_load_dwordx4 v251, %[srdWo], s64 offen offset:0 lds\n\t"
    "ds_read_b64_tr_b16 v[94:95], v242 offset:2048\n\t"
    "v_mfma_f32_16x16x32_bf16 v[228:231], v[84:87], v[76:79], v[228:231]\n\t"
    "ds_read_b64_tr_b16 v[84:85], v241 offset:0\n\t"
    "ds_read_b64_tr_b16 v[86:87], v241 offset:2048\n\t"
    "v_mfma_f32_16x16x32_bf16 v[48:51], v[100:103], v[16:19], v[48:51]\n\t"
    "ds_read_b64_tr_b16 v[16:17], v240 offset:0\n\t"
    "ds_read_b64_tr_b16 v[18:19], v240 offset:2048\n\t"
    "ds_read_b64_tr_b16 v[96:97], v236 offset:0\n\t"
    "ds_read_b64_tr_b16 v[98:99], v236 offset:2048\n\t"
    "v_mfma_f32_16x16x32_bf16 v[56:59], v[100:103], v[28:31], v[56:59]\n\t"
    "buffer_load_dwordx4 v255, %[srdWo], s64 offen offset:1024 lds\n\t"
    "v_mfma_f32_16x16x32_bf16 v[60:63], v[100:103], v[32:35], v[60:63]\n\t"
    "v_mfma_f32_16x16x32_bf16 v[64:67], v[100:103], v[36:39], v[64:67]\n\t"
    "v_mfma_f32_16x16x32_bf16 v[68:71], v[100:103], v[76:79], v[68:71]\n\t"
    "buffer_load_dwordx4 v250, %[srdWo], s64 offen offset:2048 lds\n\t"
    "s_waitcnt lgkmcnt(0)\n\t"
    "s_barrier\n\t"
    "v_mfma_f32_16x16x32_bf16 v[180:183], v[92:95], v[0:3], v[180:183]\n\t"
    "buffer_load_dwordx4 v249, %[srdWo], s64 offen offset:3072 lds\n\t"
    "ds_write_b32 v252, v52 offset:0\n\t"
    "v_mfma_f32_16x16x32_bf16 v[40:43], v[84:87], v[0:3], v[40:43]\n\t"
    "s_add_u32 m0, s65, 0\n\t"
    "s_add_u32 s64, s81, 114688\n\t"
    "buffer_load_dwordx4 v251, %[srdWo], s64 offen offset:0 lds\n\t"
    "v_mfma_f32_16x16x32_bf16 v[184:187], v[92:95], v[12:15], v[184:187]\n\t"
    "ds_write_b32 v252, v4 offset:256\n\t"
    "ds_write_b32 v252, v8 offset:512\n\t"
    "v_mfma_f32_16x16x32_bf16 v[200:203], v[84:87], v[12:15], v[200:203]\n\t"
    "buffer_load_dwordx4 v255, %[srdWo], s64 offen offset:1024 lds\n\t"
    "ds_write_b32 v252, v20 offset:768\n\t"
    "v_mfma_f32_16x16x32_bf16 v[188:191], v[92:95], v[72:75], v[188:191]\n\t"
    "ds_write_b32 v252, v24 offset:1024\n\t"
    "v_mfma_f32_16x16x32_bf16 v[204:207], v[84:87], v[72:75], v[204:207]\n\t"
    "s_waitcnt vmcnt(14)\n\t"
    "ds_read_b64_tr_b16 v[4:5], v242 offset:4096\n\t"
    "ds_read_b64_tr_b16 v[6:7], v242 offset:6144\n\t"
    "v_mfma_f32_16x16x32_bf16 v[192:195], v[92:95], v[80:83], v[192:195]\n\t"
    "ds_read_b64_tr_b16 v[8:9], v241 offset:4096\n\t"
    "ds_read_b64_tr_b16 v[10:11], v241 offset:6144\n\t"
    "v_mfma_f32_16x16x32_bf16 v[208:211], v[84:87], v[80:83], v[208:211]\n\t"
    "buffer_load_dwordx4 v250, %[srdWo], s64 offen offset:2048 lds\n\t"
    "ds_read_b64_tr_b16 a[0:1], v240 offset:4096\n\t"
    "v_mfma_f32_16x16x32_bf16 v[196:199], v[92:95], v[88:91], v[196:199]\n\t"
    "ds_read_b64_tr_b16 a[2:3], v240 offset:6144\n\t"
    "ds_read_b64_tr_b16 a[4:5], v236 offset:4096\n\t"
    "v_mfma_f32_16x16x32_bf16 v[212:215], v[84:87], v[88:91], v[212:215]\n\t"
    "ds_read_b64_tr_b16 a[6:7], v236 offset:6144\n\t"
    "v_cvt_pk_bf16_f32 v20, v180, v181\n\t"
    "v_mfma_f32_16x16x32_bf16 v[44:47], v[16:19], v[0:3], v[44:47]\n\t"
    "buffer_load_dwordx4 v249, %[srdWo], s64 offen offset:3072 lds\n\t"
    "v_cvt_pk_bf16_f32 v21, v182, v183\n\t"
    "v_mfma_f32_16x16x32_bf16 v[48:51], v[96:99], v[0:3], v[48:51]\n\t"
    "v_cvt_pk_bf16_f32 v22, v40, v41\n\t"
    "v_cvt_pk_bf16_f32 v23, v42, v43\n\t"
    "v_mfma_f32_16x16x32_bf16 v[216:219], v[16:19], v[12:15], v[216:219]\n\t"
    "ds_write_b128 v235, v[20:23] offset:2048\n\t"
    "v_cvt_pk_bf16_f32 v0, v184, v185\n\t"
    "v_mfma_f32_16x16x32_bf16 v[56:59], v[96:99], v[12:15], v[56:59]\n\t"
    "v_cvt_pk_bf16_f32 v1, v186, v187\n\t"
    "v_cvt_pk_bf16_f32 v2, v200, v201\n\t"
    "v_mfma_f32_16x16x32_bf16 v[220:223], v[16:19], v[72:75], v[220:223]\n\t"
    "s_add_u32 m0, s65, 4096\n\t"
    "s_add_u32 s64, s81, 143360\n\t"
    "s_waitcnt lgkmcnt(1)\n\t"
    "buffer_load_dwordx4 v251, %[srdWo], s64 offen offset:0 lds\n\t"
    "v_mfma_f32_16x16x32_bf16 v[60:63], v[96:99], v[72:75], v[60:63]\n\t"
    "v_cvt_pk_bf16_f32 v3, v202, v203\n\t"
    "ds_write_b128 v235, v[0:3] offset:3072\n\t"
    "v_mfma_f32_16x16x32_bf16 v[224:227], v[16:19], v[80:83], v[224:227]\n\t"
    "s_nop 0\n\t"
    "v_cvt_pk_bf16_f32 v0, v188, v189\n\t"
    "v_cvt_pk_bf16_f32 v1, v190, v191\n\t"
    "v_mfma_f32_16x16x32_bf16 v[64:67], v[96:99], v[80:83], v[64:67]\n\t"
    "buffer_load_dwordx4 v255, %[srdWo], s64 offen offset:1024 lds\n\t"
    "v_cvt_pk_bf16_f32 v2, v204, v205\n\t"
    "v_mfma_f32_16x16x32_bf16 v[228:231], v[16:19], v[88:91], v[228:231]\n\t"
    "v_cvt_pk_bf16_f32 v3, v206, v207\n\t"
    "ds_write_b128 v235, v[0:3] offset:4096\n\t"
    "v_mfma_f32_16x16x32_bf16 v[68:71], v[96:99], v[88:91], v[68:71]\n\t"
    "s_nop 0\n\t"
    "v_cvt_pk_bf16_f32 v0, v192, v193\n\t"
    "v_cvt_pk_bf16_f32 v1, v194, v195\n\t"
    "v_cvt_pk_bf16_f32 v2, v208, v209\n\t"
    "v_cvt_pk_bf16_f32 v3, v210, v211\n\t"
    "ds_write_b128 v235, v[0:3] offset:5120\n\t"
    "s_nop 1\n\t"
    "v_cvt_pk_bf16_f32 v0, v196, v197\n\t"
    "v_cvt_pk_bf16_f32 v1, v198, v199\n\t"
    "v_cvt_pk_bf16_f32 v2, v212, v213\n\t"
    "v_cvt_pk_bf16_f32 v3, v214, v215\n\t"
    "ds_write_b128 v235, v[0:3] offset:6144\n\t"
    "s_nop 1\n\t"
    "v_cvt_pk_bf16_f32 v0, v44, v45\n\t"
    "v_cvt_pk_bf16_f32 v1, v46, v47\n\t"
    "v_cvt_pk_bf16_f32 v2, v48, v49\n\t"
    "v_cvt_pk_bf16_f32 v3, v50, v51\n\t"
    "ds_write_b128 v235, v[0:3] offset:7168\n\t"
    "s_nop 1\n\t"
    "v_cvt_pk_bf16_f32 v0, v216, v217\n\t"
    "v_cvt_pk_bf16_f32 v1, v218, v219\n\t"
    "v_cvt_pk_bf16_f32 v2, v56, v57\n\t"
    "v_cvt_pk_bf16_f32 v3, v58, v59\n\t"
    "ds_write_b128 v235, v[0:3] offset:8192\n\t"
    "s_nop 1\n\t"
    "v_cvt_pk_bf16_f32 v0, v220, v221\n\t"
    "v_cvt_pk_bf16_f32 v1, v222, v223\n\t"
    "v_cvt_pk_bf16_f32 v2, v60, v61\n\t"
    "v_cvt_pk_bf16_f32 v3, v62, v63\n\t"
    "ds_write_b128 v235, v[0:3] offset:9216\n\t"
    "s_nop 1\n\t"
    "v_cvt_pk_bf16_f32 v0, v224, v225\n\t"
    "v_cvt_pk_bf16_f32 v1, v226, v227\n\t"
    "v_cvt_pk_bf16_f32 v2, v64, v65\n\t"
    "v_cvt_pk_bf16_f32 v3, v66, v67\n\t"
    "ds_write_b128 v235, v[0:3] offset:10240\n\t"
    "s_nop 1\n\t"
    "v_cvt_pk_bf16_f32 v0, v228, v229\n\t"
    "v_cvt_pk_bf16_f32 v1, v230, v231\n\t"
    "v_cvt_pk_bf16_f32 v2, v68, v69\n\t"
    "v_cvt_pk_bf16_f32 v3, v70, v71\n\t"
    "ds_write_b128 v235, v[0:3] offset:11264\n\t"
    "s_waitcnt lgkmcnt(0)\n\t"
    "s_barrier\n\t"
    "ds_read_b128 a[8:11], v254 offset:2048\n\t"
    "ds_read_b128 a[12:15], v254 offset:3072\n\t"
    "ds_read_b128 a[16:19], v254 offset:4096\n\t"
    "ds_read_b128 a[20:23], v254 offset:5120\n\t"
    "ds_read_b128 a[24:27], v254 offset:6144\n\t"
    "ds_read_b128 a[28:31], v254 offset:7168\n\t"
    "ds_read_b128 a[32:35], v254 offset:8192\n\t"
    "ds_read_b128 a[36:39], v254 offset:9216\n\t"
    "ds_read_b128 a[40:43], v254 offset:10240\n\t"
    "ds_read_b128 a[44:47], v254 offset:11264\n\t"
    "buffer_load_dwordx4 v250, %[srdWo], s64 offen offset:2048 lds\n\t"
    "buffer_load_dwordx4 v249, %[srdWo], s64 offen offset:3072 lds\n\t"
    "s_waitcnt vmcnt(16)\n\t"
    "ds_read_b64_tr_b16 v[0:1], v242 offset:8192\n\t"
    "ds_read_b64_tr_b16 v[2:3], v242 offset:10240\n\t"
    "ds_read_b64_tr_b16 v[12:13], v241 offset:8192\n\t"
    "ds_read_b64_tr_b16 v[14:15], v241 offset:10240\n\t"
    "ds_read_b64_tr_b16 a[48:49], v240 offset:8192\n\t"
    "ds_read_b64_tr_b16 a[50:51], v240 offset:10240\n\t"
    "ds_read_b64_tr_b16 a[52:53], v236 offset:8192\n\t"
    "ds_read_b64_tr_b16 a[54:55], v236 offset:10240\n\t"
    "s_waitcnt lgkmcnt(14)\n\t"
    "v_mfma_f32_16x16x32_bf16 v[16:19], v[4:7], a[8:11], 0\n\t"
    "s_add_u32 m0, s65, 8192\n\t"
    "s_add_u32 s64, s81, 172032\n\t"
    "s_waitcnt lgkmcnt(0)\n\t"
    "buffer_load_dwordx4 v251, %[srdWo], s64 offen offset:0 lds\n\t"
    "buffer_load_dwordx4 v255, %[srdWo], s64 offen offset:1024 lds\n\t"
    "v_mfma_f32_16x16x32_bf16 v[20:23], v[4:7], a[12:15], 0\n\t"
    "buffer_load_dwordx4 v250, %[srdWo], s64 offen offset:2048 lds\n\t"
    "buffer_load_dwordx4 v249, %[srdWo], s64 offen offset:3072 lds\n\t"
    "ds_read_b128 a[56:59], v254 offset:22528\n\t"
    "s_waitcnt vmcnt(16)\n\t"
    "ds_read_b64_tr_b16 v[24:25], v242 offset:12288\n\t"
    "v_mfma_f32_16x16x32_bf16 v[28:31], v[4:7], a[16:19], 0\n\t"
    "ds_read_b64_tr_b16 v[26:27], v242 offset:14336\n\t"
    "ds_read_b64_tr_b16 v[32:33], v241 offset:12288\n\t"
    "ds_read_b64_tr_b16 v[34:35], v241 offset:14336\n\t"
    "ds_read_b64_tr_b16 a[60:61], v240 offset:12288\n\t"
    "v_mfma_f32_16x16x32_bf16 v[36:39], v[4:7], a[20:23], 0\n\t"
    "ds_read_b128 a[64:67], v254 offset:23552\n\t"
    "ds_read_b64_tr_b16 a[62:63], v240 offset:14336\n\t"
    "ds_read_b64_tr_b16 a[68:69], v236 offset:12288\n\t"
    "ds_read_b64_tr_b16 a[70:71], v236 offset:14336\n\t"
    "v_mfma_f32_16x16x32_bf16 v[40:43], v[4:7], a[24:27], 0\n\t"
    "s_waitcnt vmcnt(12)\n\t"
    "ds_read_b64_tr_b16 v[4:5], v242 offset:16384\n\t"
    "ds_read_b64_tr_b16 v[6:7], v242 offset:18432\n\t"
    "ds_read_b64_tr_b16 v[44:45], v241 offset:16384\n\t"
    "ds_read_b64_tr_b16 v[46:47], v241 offset:18432\n\t"
    "v_mfma_f32_16x16x32_bf16 v[48:51], v[8:11], a[8:11], 0\n\t"
    "ds_read_b128 a[72:75], v254 offset:24576\n\t"
    "ds_read_b64_tr_b16 a[76:77], v240 offset:16384\n\t"
    "ds_read_b64_tr_b16 a[78:79], v240 offset:18432\n\t"
    "ds_read_b64_tr_b16 a[80:81], v236 offset:16384\n\t"
    "v_mfma_f32_16x16x32_bf16 v[52:55], v[8:11], a[12:15], 0\n\t"
    "s_add_u32 m0, s65, 12288\n\t"
    "s_add_u32 s64, s81, 200704\n\t"
    "s_waitcnt lgkmcnt(8)\n\t"
    "buffer_load_dwordx4 v251, %[srdWo], s64 offen offset:0 lds\n\t"
    "buffer_load_dwordx4 v255, %[srdWo], s64 offen offset:1024 lds\n\t"
    "v_mfma_f32_16x16x32_bf16 v[56:59], v[8:11], a[16:19], 0\n\t"
    "buffer_load_dwordx4 v250, %[srdWo], s64 offen offset:2048 lds\n\t"
    "buffer_load_dwordx4 v249, %[srdWo], s64 offen offset:3072 lds\n\t"
    "ds_read_b128 a[84:87], v254 offset:25600\n\t"
    "ds_read_b64_tr_b16 a[82:83], v236 offset:18432\n\t"
    "v_mfma_f32_16x16x32_bf16 v[60:63], v[8:11], a[20:23], 0\n\t"
    "v_mfma_f32_16x16x32_bf16 v[64:67], v[8:11], a[24:27], 0\n\t"
    "ds_read_b128 a[88:91], v254 offset:26624\n\t"
    "v_mfma_f32_16x16x32_bf16 v[16:19], v[0:3], a[28:31], v[16:19]\n\t"
    "v_mfma_f32_16x16x32_bf16 v[20:23], v[0:3], a[32:35], v[20:23]\n\t"
    "v_mbcnt_lo_u32_b32 v255, -1, 0\n\t"
    "v_mbcnt_hi_u32_b32 v255, -1, v255\n\t"
    "v_lshrrev_b32 v252, 3, v255\n\t"
    "v_and_b32 v251, 6, v252\n\t"
    "v_xor_b32 v251, v251, v255\n\t"
    "v_and_b32 v251, 7, v251\n\t"
    "v_mul_u32_u24 v255, 896, v252\n\t"
    "v_min_u32 v251, 5, v251\n\t"
    "v_lshl_add_u32 v252, v251, 4, v255\n\t"
    "v_add_u32 v255, 6144, v252\n\t"
    "v_add_u32 v251, 12288, v252\n\t"
    "v_add_u32 v250, 18432, v252\n\t"
    "s_add_u32 m0, s65, 16384\n\t"
    "s_add_u32 s64, s97, 0\n\t"
    "s_waitcnt lgkmcnt(1)\n\t"
    "buffer_load_dwordx4 v252, %[srdWo], s64 offen offset:0 lds\n\t"
    "v_mfma_f32_16x16x32_bf16 v[28:31], v[0:3], a[36:39], v[28:31]\n\t"
    "buffer_load_dwordx4 v255, %[srdWo], s64 offen offset:1024 lds\n\t"
    "buffer_load_dwordx4 v251, %[srdWo], s64 offen offset:2048 lds\n\t"
    "ds_read_b128 a[92:95], v254 offset:27648\n\t"
    "s_waitcnt vmcnt(15)\n\t"
    "ds_read_b64_tr_b16 v[8:9], v242 offset:0\n\t"
    "v_mfma_f32_16x16x32_bf16 v[36:39], v[0:3], a[40:43], v[36:39]\n\t"
    "ds_read_b128 a[96:99], v254 offset:28672\n\t"
    "ds_read_b128 a[100:103], v254 offset:29696\n\t"
    "ds_read_b128 a[104:107], v254 offset:30720\n\t"
    "ds_read_b64_tr_b16 v[10:11], v242 offset:2048\n\t"
    "v_mfma_f32_16x16x32_bf16 v[40:43], v[0:3], a[44:47], v[40:43]\n\t"
    "ds_read_b128 a[108:111], v254 offset:31744\n\t"
    "ds_read_b64_tr_b16 v[0:1], v241 offset:0\n\t"
    "ds_read_b64_tr_b16 v[2:3], v241 offset:2048\n\t"
    "ds_read_b64_tr_b16 a[112:113], v240 offset:0\n\t"
    "v_mfma_f32_16x16x32_bf16 v[48:51], v[12:15], a[28:31], v[48:51]\n\t"
    "ds_read_b64_tr_b16 a[114:115], v240 offset:2048\n\t"
    "ds_read_b64_tr_b16 a[116:117], v236 offset:0\n\t"
    "ds_read_b64_tr_b16 a[118:119], v236 offset:2048\n\t"
    "v_mfma_f32_16x16x32_bf16 v[52:55], v[12:15], a[32:35], v[52:55]\n\t"
    "v_mfma_f32_16x16x32_bf16 v[56:59], v[12:15], a[36:39], v[56:59]\n\t"
    "v_mfma_f32_16x16x32_bf16 v[60:63], v[12:15], a[40:43], v[60:63]\n\t"
    "v_mfma_f32_16x16x32_bf16 v[64:67], v[12:15], a[44:47], v[64:67]\n\t"
    "v_mfma_f32_16x16x32_bf16 v[16:19], v[24:27], a[56:59], v[16:19]\n\t"
    "buffer_load_dwordx4 v250, %[srdWo], s64 offen offset:3072 lds\n\t"
    "ds_read_b128 a[120:123], v253 offset:2048\n\t"
    "v_mfma_f32_16x16x32_bf16 v[20:23], v[24:27], a[64:67], v[20:23]\n\t"
    "v_mfma_f32_16x16x32_bf16 v[28:31], v[24:27], a[72:75], v[28:31]\n\t"
    "ds_read_b128 a[124:127], v253 offset:3072\n\t"
    "v_mfma_f32_16x16x32_bf16 v[36:39], v[24:27], a[84:87], v[36:39]\n\t"
    "s_add_u32 m0, s65, 0\n\t"
    "s_add_u32 s64, s97, 28672\n\t"
    "s_waitcnt lgkmcnt(2)\n\t"
    "buffer_load_dwordx4 v252, %[srdWo], s64 offen offset:0 lds\n\t"
    "v_mfma_f32_16x16x32_bf16 v[40:43], v[24:27], a[88:91], v[40:43]\n\t"
    "ds_read_b128 a[128:131], v253 offset:4096\n\t"
    "v_mfma_f32_16x16x32_bf16 v[48:51], v[32:35], a[56:59], v[48:51]\n\t"
    "v_mfma_f32_16x16x32_bf16 v[52:55], v[32:35], a[64:67], v[52:55]\n\t"
    "ds_read_b128 a[132:135], v253 offset:5120\n\t"
    "v_mfma_f32_16x16x32_bf16 v[56:59], v[32:35], a[72:75], v[56:59]\n\t"
    "buffer_load_dwordx4 v255, %[srdWo], s64 offen offset:1024 lds\n\t"
    "v_mfma_f32_16x16x32_bf16 v[60:63], v[32:35], a[84:87], v[60:63]\n\t"
    "ds_read_b128 a[136:139], v253 offset:6144\n\t"
    "s_waitcnt vmcnt(14)\n\t"
    "ds_read_b64_tr_b16 v[12:13], v242 offset:4096\n\t"
    "ds_read_b64_tr_b16 v[14:15], v242 offset:6144\n\t"
    "ds_read_b64_tr_b16 v[24:25], v241 offset:4096\n\t"
    "v_mfma_f32_16x16x32_bf16 v[64:67], v[32:35], a[88:91], v[64:67]\n\t"
    "buffer_load_dwordx4 v251, %[srdWo], s64 offen offset:2048 lds\n\t"
    "ds_read_b64_tr_b16 v[26:27], v241 offset:6144\n\t"
    "ds_read_b64_tr_b16 a[140:141], v240 offset:4096\n\t"
    "ds_read_b64_tr_b16 a[142:143], v240 offset:6144\n\t"
    "v_mfma_f32_16x16x32_bf16 v[16:19], v[4:7], a[92:95], v[16:19]\n\t"
    "ds_read_b128 a[144:147], v253 offset:7168\n\t"
    "ds_read_b64_tr_b16 a[148:149], v236 offset:4096\n\t"
    "ds_read_b64_tr_b16 a[150:151], v236 offset:6144\n\t"
    "v_mfma_f32_16x16x32_bf16 v[20:23], v[4:7], a[96:99], v[20:23]\n\t"
    "v_mfma_f32_16x16x32_bf16 v[28:31], v[4:7], a[100:103], v[28:31]\n\t"
    "buffer_load_dwordx4 v250, %[srdWo], s64 offen offset:3072 lds\n\t"
    "ds_read_b128 a[152:155], v253 offset:8192\n\t"
    "v_mfma_f32_16x16x32_bf16 v[36:39], v[4:7], a[104:107], v[36:39]\n\t"
    "v_mfma_f32_16x16x32_bf16 v[40:43], v[4:7], a[108:111], v[40:43]\n\t"
    "ds_read_b128 a[156:159], v253 offset:9216\n\t"
    "ds_read_b128 a[160:163], v253 offset:10240\n\t"
    "v_mfma_f32_16x16x32_bf16 v[48:51], v[44:47], a[92:95], v[48:51]\n\t"
    "s_add_u32 m0, s65, 4096\n\t"
    "s_add_u32 s64, s97, 57344\n\t"
    "s_waitcnt lgkmcnt(3)\n\t"
    "buffer_load_dwordx4 v252, %[srdWo], s64 offen offset:0 lds\n\t"
    "ds_read_b128 a[164:167], v253 offset:11264\n\t"
    "v_mfma_f32_16x16x32_bf16 v[52:55], v[44:47], a[96:99], v[52:55]\n\t"
    "ds_read_b128 v[4:7], v234 offset:0\n\t"
    "ds_read_b128 v[32:35], v234 offset:256\n\t"
    "ds_read_b128 v[68:71], v234 offset:512\n\t"
    "v_mfma_f32_16x16x32_bf16 v[56:59], v[44:47], a[100:103], v[56:59]\n\t"
    "ds_read_b128 v[72:75], v234 offset:768\n\t"
    "ds_read_b128 v[76:79], v234 offset:1024\n\t"
    "v_mfma_f32_16x16x32_bf16 v[60:63], v[44:47], a[104:107], v[60:63]\n\t"
    "buffer_load_dwordx4 v255, %[srdWo], s64 offen offset:1024 lds\n\t"
    "v_mfma_f32_16x16x32_bf16 v[64:67], v[44:47], a[108:111], v[64:67]\n\t"
    "v_mfma_f32_16x16x32_bf16 v[16:19], v[8:11], a[120:123], v[16:19]\n\t"
    "ds_read_b128 a[168:171], v253 offset:22528\n\t"
    "s_waitcnt vmcnt(14)\n\t"
    "ds_read_b64_tr_b16 v[44:45], v242 offset:8192\n\t"
    "ds_read_b64_tr_b16 v[46:47], v242 offset:10240\n\t"
    "ds_read_b64_tr_b16 v[80:81], v241 offset:8192\n\t"
    "v_mfma_f32_16x16x32_bf16 v[20:23], v[8:11], a[124:127], v[20:23]\n\t"
    "buffer_load_dwordx4 v251, %[srdWo], s64 offen offset:2048 lds\n\t"
    "ds_read_b64_tr_b16 v[82:83], v241 offset:10240\n\t"
    "ds_read_b64_tr_b16 a[172:173], v240 offset:8192\n\t"
    "ds_read_b64_tr_b16 a[174:175], v240 offset:10240\n\t"
    "v_mfma_f32_16x16x32_bf16 v[28:31], v[8:11], a[128:131], v[28:31]\n\t"
    "ds_read_b128 a[176:179], v253 offset:23552\n\t"
    "ds_read_b64_tr_b16 a[180:181], v236 offset:8192\n\t"
    "ds_read_b64_tr_b16 a[182:183], v236 offset:10240\n\t"
    "v_mfma_f32_16x16x32_bf16 v[36:39], v[8:11], a[132:135], v[36:39]\n\t"
    "buffer_load_dwordx4 v250, %[srdWo], s64 offen offset:3072 lds\n\t"
    "s_waitcnt lgkmcnt(14)\n\t"
    "v_add_f32 v4, v4, v5\n\t"
    "v_mfma_f32_16x16x32_bf16 v[40:43], v[8:11], a[136:139], v[40:43]\n\t"
    "ds_read_b128 a[184:187], v253 offset:24576\n\t"
    "s_waitcnt lgkmcnt(14)\n\t"
    "v_add_f32 v32, v32, v33\n\t"
    "v_mfma_f32_16x16x32_bf16 v[48:51], v[0:3], a[120:123], v[48:51]\n\t"
    "s_waitcnt lgkmcnt(13)\n\t"
    "v_add_f32 v68, v68, v69\n\t"
    "s_waitcnt lgkmcnt(12)\n\t"
    "v_add_f32 v72, v72, v73\n\t"
    "v_mfma_f32_16x16x32_bf16 v[52:55], v[0:3], a[124:127], v[52:55]\n\t"
    "s_add_u32 m0, s65, 8192\n\t"
    "s_add_u32 s64, s97, 86016\n\t"
    "s_waitcnt lgkmcnt(1)\n\t"
    "buffer_load_dwordx4 v252, %[srdWo], s64 offen offset:0 lds\n\t"
    "ds_read_b128 a[188:191], v253 offset:25600\n\t"
    "v_mfma_f32_16x16x32_bf16 v[56:59], v[0:3], a[128:131], v[56:59]\n\t"
    "v_add_f32 v76, v76, v77\n\t"
    "v_add_f32 v6, v6, v7\n\t"
    "v_mfma_f32_16x16x32_bf16 v[60:63], v[0:3], a[132:135], v[60:63]\n\t"
    "buffer_load_dwordx4 v255, %[srdWo], s64 offen offset:1024 lds\n\t"
    "ds_read_b128 a[192:195], v253 offset:26624\n\t"
    "v_mfma_f32_16x16x32_bf16 v[64:67], v[0:3], a[136:139], v[64:67]\n\t"
    "v_add_f32 v34, v34, v35\n\t"
    "v_add_f32 v70, v70, v71\n\t"
    "v_mfma_f32_16x16x32_bf16 v[16:19], v[12:15], a[144:147], v[16:19]\n\t"
    "ds_read_b128 a[196:199], v253 offset:27648\n\t"
    "v_add_f32 v74, v74, v75\n\t"
    "v_mfma_f32_16x16x32_bf16 v[20:23], v[12:15], a[152:155], v[20:23]\n\t"
    "s_waitcnt vmcnt(14)\n\t"
    "ds_read_b64_tr_b16 v[0:1], v242 offset:12288\n\t"
    "ds_read_b64_tr_b16 v[2:3], v242 offset:14336\n\t"
    "ds_read_b64_tr_b16 v[8:9], v241 offset:12288\n\t"
    "ds_read_b64_tr_b16 v[10:11], v241 offset:14336\n\t"
    "v_mfma_f32_16x16x32_bf16 v[28:31], v[12:15], a[156:159], v[28:31]\n\t"
    "buffer_load_dwordx4 v251, %[srdWo], s64 offen offset:2048 lds\n\t"
    "ds_read_b128 a[200:203], v253 offset:28672\n\t"
    "ds_read_b64_tr_b16 a[204:205], v240 offset:12288\n\t"
    "ds_read_b64_tr_b16 a[206:207], v240 offset:14336\n\t"
    "v_mfma_f32_16x16x32_bf16 v[36:39], v[12:15], a[160:163], v[36:39]\n\t"
    "ds_read_b64_tr_b16 a[208:209], v236 offset:12288\n\t"
    "ds_read_b64_tr_b16 a[210:211], v236 offset:14336\n\t"
    "v_mfma_f32_16x16x32_bf16 v[40:43], v[12:15], a[164:167], v[40:43]\n\t"
    "buffer_load_dwordx4 v250, %[srdWo], s64 offen offset:3072 lds\n\t"
    "ds_read_b128 a[212:215], v253 offset:29696\n\t"
    "v_mfma_f32_16x16x32_bf16 v[48:51], v[24:27], a[144:147], v[48:51]\n\t"
    "v_add_f32 v78, v78, v79\n\t"
    "v_add_f32 v254, v4, v6\n\t"
    "v_mfma_f32_16x16x32_bf16 v[52:55], v[24:27], a[152:155], v[52:55]\n\t"
    "ds_read_b128 a[216:219], v253 offset:30720\n\t"
    "v_add_f32 v249, v32, v34\n\t"
    "v_mfma_f32_16x16x32_bf16 v[56:59], v[24:27], a[156:159], v[56:59]\n\t"
    "s_add_u32 m0, s65, 12288\n\t"
    "s_add_u32 s64, s97, 114688\n\t"
    "s_waitcnt lgkmcnt(2)\n\t"
    "buffer_load_dwordx4 v252, %[srdWo], s64 offen offset:0 lds\n\t"
    "ds_read_b128 a[220:223], v253 offset:31744\n\t"
    "v_mfma_f32_16x16x32_bf16 v[60:63], v[24:27], a[160:163], v[60:63]\n\t"
    "v_add_f32 v253, v68, v70\n\t"
    "v_add_f32 v248, v72, v74\n\t"
    "v_mfma_f32_16x16x32_bf16 v[64:67], v[24:27], a[164:167], v[64:67]\n\t"
    "v_add_f32 v245, v76, v78\n\t"
    "v_rcp_f32 v254, v254\n\t"
    "v_rcp_f32 v249, v249\n\t"
    "v_rcp_f32 v253, v253\n\t"
    "v_rcp_f32 v248, v248\n\t"
    "v_rcp_f32 v245, v245\n\t"
    "v_mfma_f32_16x16x32_bf16 v[16:19], v[44:47], a[168:171], v[16:19]\n\t"
    "buffer_load_dwordx4 v255, %[srdWo], s64 offen offset:1024 lds\n\t"
    "s_waitcnt vmcnt(14)\n\t"
    "ds_read_b64_tr_b16 a[224:225], v242 offset:16384\n\t"
    "ds_read_b64_tr_b16 a[226:227], v242 offset:18432\n\t"
    "ds_read_b64_tr_b16 a[228:229], v241 offset:16384\n\t"
    "v_mfma_f32_16x16x32_bf16 v[20:23], v[44:47], a[176:179], v[20:23]\n\t"
    "buffer_load_dwordx4 v251, %[srdWo], s64 offen offset:2048 lds\n\t"
    "ds_read_b64_tr_b16 a[230:231], v241 offset:18432\n\t"
    "ds_read_b64_tr_b16 a[232:233], v240 offset:16384\n\t"
    "ds_read_b64_tr_b16 a[234:235], v240 offset:18432\n\t"
    "v_mfma_f32_16x16x32_bf16 v[28:31], v[44:47], a[184:187], v[28:31]\n\t"
    "v_mfma_f32_16x16x32_bf16 v[36:39], v[44:47], a[188:191], v[36:39]\n\t"
    "buffer_load_dwordx4 v250, %[srdWo], s64 offen offset:3072 lds\n\t"
    "v_mfma_f32_16x16x32_bf16 v[40:43], v[44:47], a[192:195], v[40:43]\n\t"
    "v_mfma_f32_16x16x32_bf16 v[48:51], v[80:83], a[168:171], v[48:51]\n\t"
    "v_mfma_f32_16x16x32_bf16 v[52:55], v[80:83], a[176:179], v[52:55]\n\t"
    "s_add_u32 m0, s65, 16384\n\t"
    "s_add_u32 s64, s97, 143360\n\t"
    "s_waitcnt lgkmcnt(0)\n\t"
    "buffer_load_dwordx4 v252, %[srdWo], s64 offen offset:0 lds\n\t"
    "v_mfma_f32_16x16x32_bf16 v[56:59], v[80:83], a[184:187], v[56:59]\n\t"
    "v_mfma_f32_16x16x32_bf16 v[60:63], v[80:83], a[188:191], v[60:63]\n\t"
    "v_mfma_f32_16x16x32_bf16 v[64:67], v[80:83], a[192:195], v[64:67]\n\t"
    "buffer_load_dwordx4 v255, %[srdWo], s64 offen offset:1024 lds\n\t"
    "v_mfma_f32_16x16x32_bf16 v[16:19], v[0:3], a[196:199], v[16:19]\n\t"
    "v_mfma_f32_16x16x32_bf16 v[20:23], v[0:3], a[200:203], v[20:23]\n\t"
    "s_waitcnt vmcnt(14)\n\t"
    "ds_read_b64_tr_b16 a[236:237], v242 offset:0\n\t"
    "ds_read_b64_tr_b16 a[238:239], v242 offset:2048\n\t"
    "ds_read_b64_tr_b16 a[240:241], v241 offset:0\n\t"
    "ds_read_b64_tr_b16 a[242:243], v241 offset:2048\n\t"
    "v_mfma_f32_16x16x32_bf16 v[28:31], v[0:3], a[212:215], v[28:31]\n\t"
    "buffer_load_dwordx4 v251, %[srdWo], s64 offen offset:2048 lds\n\t"
    "ds_read_b64_tr_b16 a[244:245], v240 offset:0\n\t"
    "ds_read_b64_tr_b16 a[246:247], v240 offset:2048\n\t"
    "v_mfma_f32_16x16x32_bf16 v[36:39], v[0:3], a[216:219], v[36:39]\n\t"
    "v_mfma_f32_16x16x32_bf16 v[40:43], v[0:3], a[220:223], v[40:43]\n\t"
    "v_mfma_f32_16x16x32_bf16 v[48:51], v[8:11], a[196:199], v[48:51]\n\t"
    "buffer_load_dwordx4 v250, %[srdWo], s64 offen offset:3072 lds\n\t"
    "v_mfma_f32_16x16x32_bf16 v[52:55], v[8:11], a[200:203], v[52:55]\n\t"
    "v_mfma_f32_16x16x32_bf16 v[56:59], v[8:11], a[212:215], v[56:59]\n\t"
    "v_mfma_f32_16x16x32_bf16 v[60:63], v[8:11], a[216:219], v[60:63]\n\t"
    "s_add_u32 m0, s65, 0\n\t"
    "s_add_u32 s64, s97, 172032\n\t"
    "s_waitcnt lgkmcnt(0)\n\t"
    "buffer_load_dwordx4 v252, %[srdWo], s64 offen offset:0 lds\n\t"
    "v_mfma_f32_16x16x32_bf16 v[64:67], v[8:11], a[220:223], v[64:67]\n\t"
    "v_mfma_f32_16x16x32_bf16 v[0:3], a[0:3], a[8:11], 0\n\t"
    "v_mfma_f32_16x16x32_bf16 v[4:7], a[0:3], a[12:15], 0\n\t"
    "buffer_load_dwordx4 v255, %[srdWo], s64 offen offset:1024 lds\n\t"
    "v_mfma_f32_16x16x32_bf16 v[8:11], a[0:3], a[16:19], 0\n\t"
    "v_mfma_f32_16x16x32_bf16 v[12:15], a[0:3], a[20:23], 0\n\t"
    "s_waitcnt vmcnt(14)\n\t"
    "ds_read_b64_tr_b16 a[248:249], v242 offset:4096\n\t"
    "ds_read_b64_tr_b16 a[250:251], v242 offset:6144\n\t"
    "ds_read_b64_tr_b16 a[252:253], v241 offset:4096\n\t"
    "ds_read_b64_tr_b16 a[254:255], v241 offset:6144\n\t"
    "v_mfma_f32_16x16x32_bf16 v[24:27], a[0:3], a[24:27], 0\n\t"
    "buffer_load_dwordx4 v251, %[srdWo], s64 offen offset:2048 lds\n\t"
    "ds_read_b64_tr_b16 a[0:1], v240 offset:4096\n\t"
    "ds_read_b64_tr_b16 a[2:3], v240 offset:6144\n\t"
    "v_mfma_f32_16x16x32_bf16 v[32:35], a[4:7], a[8:11], 0\n\t"
    "v_mul_f32 v16, v16, v254\n\t"
    "v_mul_f32 v17, v17, v254\n\t"
    "v_mul_f32 v18, v18, v254\n\t"
    "v_mfma_f32_16x16x32_bf16 v[44:47], a[4:7], a[12:15], 0\n\t"
    "buffer_load_dwordx4 v250, %[srdWo], s64 offen offset:3072 lds\n\t"
    "v_mul_f32 v19, v19, v254\n\t"
    "v_cvt_pk_bf16_f32 v68, v16, v17\n\t"
    "v_mfma_f32_16x16x32_bf16 v[72:75], a[4:7], a[16:19], 0\n\t"
    "v_cvt_pk_bf16_f32 v69, v18, v19\n\t"
    "v_mul_f32 v48, v48, v254\n\t"
    "v_mul_f32 v49, v49, v254\n\t"
    "v_mfma_f32_16x16x32_bf16 v[16:19], a[4:7], a[20:23], 0\n\t"
    "v_mul_f32 v50, v50, v254\n\t"
    "v_mul_f32 v51, v51, v254\n\t"
    "v_cvt_pk_bf16_f32 v70, v48, v49\n\t"
    "v_mfma_f32_16x16x32_bf16 v[76:79], a[4:7], a[24:27], 0\n\t"
    "s_add_u32 m0, s65, 4096\n\t"
    "s_add_u32 s64, s97, 200704\n\t"
    "s_waitcnt lgkmcnt(0)\n\t"
    "buffer_load_dwordx4 v252, %[srdWo], s64 offen offset:0 lds\n\t"
    "v_mfma_f32_16x16x32_bf16 v[0:3], a[48:51], a[28:31], v[0:3]\n\t"
    "v_cvt_pk_bf16_f32 v71, v50, v51\n\t"
    "v_mul_f32 v20, v20, v249\n\t"
    "v_mul_f32 v21, v21, v249\n\t"
    "v_mfma_f32_16x16x32_bf16 v[4:7], a[48:51], a[32:35], v[4:7]\n\t"
    "buffer_load_dwordx4 v255, %[srdWo], s64 offen offset:1024 lds\n\t"
    "v_mul_f32 v22, v22, v249\n\t"
    "v_mul_f32 v23, v23, v249\n\t"
    "v_mfma_f32_16x16x32_bf16 v[8:11], a[48:51], a[36:39], v[8:11]\n\t"
    "s_add_u32 s96, s72, 0\n\t"
    "buffer_store_dwordx4 v[68:71], v247, %[srdP], s96 offen sc1\n\t"
    "v_cvt_pk_bf16_f32 v48, v20, v21\n\t"
    "v_mfma_f32_16x16x32_bf16 v[12:15], a[48:51], a[40:43], v[12:15]\n\t"
    "s_waitcnt vmcnt(15)\n\t"
    "ds_read_b64_tr_b16 a[4:5], v242 offset:8192\n\t"
    "ds_read_b64_tr_b16 a[6:7], v242 offset:10240\n\t"
    "ds_read_b64_tr_b16 v[68:69], v241 offset:8192\n\t"
    "ds_read_b64_tr_b16 v[70:71], v241 offset:10240\n\t"
    "v_mfma_f32_16x16x32_bf16 v[24:27], a[48:51], a[44:47], v[24:27]\n\t"
    "buffer_load_dwordx4 v251, %[srdWo], s64 offen offset:2048 lds\n\t"
    "ds_read_b64_tr_b16 a[48:49], v240 offset:8192\n\t"
    "ds_read_b64_tr_b16 a[50:51], v240 offset:10240\n\t"
    "v_mfma_f32_16x16x32_bf16 v[32:35], a[52:55], a[28:31], v[32:35]\n\t"
    "v_cvt_pk_bf16_f32 v49, v22, v23\n\t"
    "v_mul_f32 v52, v52, v249\n\t"
    "v_mul_f32 v53, v53, v249\n\t"
    "v_mfma_f32_16x16x32_bf16 v[44:47], a[52:55], a[32:35], v[44:47]\n\t"
    "buffer_load_dwordx4 v250, %[srdWo], s64 offen offset:3072 lds\n\t"
    "v_mul_f32 v54, v54, v249\n\t"
    "v_mul_f32 v55, v55, v249\n\t"
    "v_mfma_f32_16x16x32_bf16 v[72:75], a[52:55], a[36:39], v[72:75]\n\t"
    "v_cvt_pk_bf16_f32 v50, v52, v53\n\t"
    "v_cvt_pk_bf16_f32 v51, v54, v55\n\t"
    "v_mul_f32 v28, v28, v253\n\t"
    "v_mfma_f32_16x16x32_bf16 v[16:19], a[52:55], a[40:43], v[16:19]\n\t"
    "s_add_u32 s96, s72, 1024\n\t"
    "buffer_store_dwordx4 v[48:51], v247, %[srdP], s96 offen sc1\n\t"
    "v_mul_f32 v29, v29, v253\n\t"
    "v_mfma_f32_16x16x32_bf16 v[76:79], a[52:55], a[44:47], v[76:79]\n\t"
    "v_mul_f32 v30, v30, v253\n\t"
    "v_mul_f32 v31, v31, v253\n\t"
    "v_cvt_pk_bf16_f32 v20, v28, v29\n\t"
    "v_mfma_f32_16x16x32_bf16 v[0:3], a[60:63], a[56:59], v[0:3]\n\t"
    "v_cvt_pk_bf16_f32 v21, v30, v31\n\t"
    "v_mul_f32 v56, v56, v253\n\t"
    "v_mul_f32 v57, v57, v253\n\t"
    "v_mfma_f32_16x16x32_bf16 v[4:7], a[60:63], a[64:67], v[4:7]\n\t"
    "v_mul_f32 v58, v58, v253\n\t"
    "v_mul_f32 v59, v59, v253\n\t"
    "v_cvt_pk_bf16_f32 v22, v56, v57\n\t"
    "v_mfma_f32_16x16x32_bf16 v[8:11], a[60:63], a[72:75], v[8:11]\n\t"
    "v_cvt_pk_bf16_f32 v23, v58, v59\n\t"
    "v_mul_f32 v36, v36, v248\n\t"
    "v_mul_f32 v37, v37, v248\n\t"
    "v_mfma_f32_16x16x32_bf16 v[12:15], a[60:63], a[84:87], v[12:15]\n\t"
    "s_waitcnt vmcnt(14)\n\t"
    "ds_read_b64_tr_b16 a[52:53], v242 offset:12288\n\t"
    "ds_read_b64_tr_b16 a[54:55], v242 offset:14336\n\t"
    "ds_read_b64_tr_b16 v[28:29], v241 offset:12288\n\t"
    "ds_read_b64_tr_b16 v[30:31], v241 offset:14336\n\t"
    "v_mfma_f32_16x16x32_bf16 v[24:27], a[60:63], a[88:91], v[24:27]\n\t"
    "ds_read_b64_tr_b16 a[60:61], v240 offset:12288\n\t"
    "ds_read_b64_tr_b16 a[62:63], v240 offset:14336\n\t"
    "v_mul_f32 v38, v38, v248\n\t"
    "v_mfma_f32_16x16x32_bf16 v[32:35], a[68:71], a[56:59], v[32:35]\n\t"
    "s_add_u32 s96, s72, 2048\n\t"
    "buffer_store_dwordx4 v[20:23], v247, %[srdP], s96 offen sc1\n\t"
    "v_mul_f32 v39, v39, v248\n\t"
    "v_mfma_f32_16x16x32_bf16 v[44:47], a[68:71], a[64:67], v[44:47]\n\t"
    "v_cvt_pk_bf16_f32 v20, v36, v37\n\t"
    "v_cvt_pk_bf16_f32 v21, v38, v39\n\t"
    "v_mul_f32 v60, v60, v248\n\t"
    "v_mfma_f32_16x16x32_bf16 v[72:75], a[68:71], a[72:75], v[72:75]\n\t"
    "v_mul_f32 v61, v61, v248\n\t"
    "v_mul_f32 v62, v62, v248\n\t"
    "v_mul_f32 v63, v63, v248\n\t"
    "v_mfma_f32_16x16x32_bf16 v[16:19], a[68:71], a[84:87], v[16:19]\n\t"
    "v_cvt_pk_bf16_f32 v22, v60, v61\n\t"
    "v_cvt_pk_bf16_f32 v23, v62, v63\n\t"
    "v_mul_f32 v40, v40, v245\n\t"
    "v_mfma_f32_16x16x32_bf16 v[76:79], a[68:71], a[88:91], v[76:79]\n\t"
    "s_add_u32 s96, s72, 3072\n\t"
    "buffer_store_dwordx4 v[20:23], v247, %[srdP], s96 offen sc1\n\t"
    "v_mul_f32 v41, v41, v245\n\t"
    "v_mfma_f32_16x16x32_bf16 v[0:3], a[76:79], a[92:95], v[0:3]\n\t"
    "v_mul_f32 v42, v42, v245\n\t"
    "v_mul_f32 v43, v43, v245\n\t"
    "v_cvt_pk_bf16_f32 v20, v40, v41\n\t"
    "v_mfma_f32_16x16x32_bf16 v[4:7], a[76:79], a[96:99], v[4:7]\n\t"
    "v_cvt_pk_bf16_f32 v21, v42, v43\n\t"
    "v_mul_f32 v64, v64, v245\n\t"
    "v_mul_f32 v65, v65, v245\n\t"
    "v_mfma_f32_16x16x32_bf16 v[8:11], a[76:79], a[100:103], v[8:11]\n\t"
    "v_mul_f32 v66, v66, v245\n\t"
    "v_mul_f32 v67, v67, v245\n\t"
    "v_cvt_pk_bf16_f32 v22, v64, v65\n\t"
    "v_mfma_f32_16x16x32_bf16 v[12:15], a[76:79], a[104:107], v[12:15]\n\t"
    "s_waitcnt vmcnt(12)\n\t"
    "ds_read_b64_tr_b16 v[36:37], v242 offset:16384\n\t"
    "ds_read_b64_tr_b16 v[38:39], v242 offset:18432\n\t"
    "ds_read_b64_tr_b16 v[40:41], v241 offset:16384\n\t"
    "ds_read_b64_tr_b16 v[42:43], v241 offset:18432\n\t"
    "v_mfma_f32_16x16x32_bf16 v[24:27], a[76:79], a[108:111], v[24:27]\n\t"
    "ds_read_b64_tr_b16 a[68:69], v240 offset:16384\n\t"
    "ds_read_b64_tr_b16 a[70:71], v240 offset:18432\n\t"
    "v_cvt_pk_bf16_f32 v23, v66, v67\n\t"
    "v_mfma_f32_16x16x32_bf16 v[32:35], a[80:83], a[92:95], v[32:35]\n\t"
    "s_add_u32 s96, s72, 4096\n\t"
    "buffer_store_dwordx4 v[20:23], v247, %[srdP], s96 offen sc1\n\t"
    "v_mfma_f32_16x16x32_bf16 v[44:47], a[80:83], a[96:99], v[44:47]\n\t"
    "v_mfma_f32_16x16x32_bf16 v[72:75], a[80:83], a[100:103], v[72:75]\n\t"
    "v_mfma_f32_16x16x32_bf16 v[16:19], a[80:83], a[104:107], v[16:19]\n\t"
    "v_mfma_f32_16x16x32_bf16 v[76:79], a[80:83], a[108:111], v[76:79]\n\t"
    "v_mfma_f32_16x16x32_bf16 v[0:3], a[112:115], a[120:123], v[0:3]\n\t"
    "v_mfma_f32_16x16x32_bf16 v[4:7], a[112:115], a[124:127], v[4:7]\n\t"
    "v_mfma_f32_16x16x32_bf16 v[8:11], a[112:115], a[128:131], v[8:11]\n\t"
    "v_mfma_f32_16x16x32_bf16 v[12:15], a[112:115], a[132:135], v[12:15]\n\t"
    "v_mfma_f32_16x16x32_bf16 v[24:27], a[112:115], a[136:139], v[24:27]\n\t"
    "v_mfma_f32_16x16x32_bf16 v[32:35], a[116:119], a[120:123], v[32:35]\n\t"
    "s_waitcnt vmcnt(9)\n\t"
    "ds_read_b64_tr_b16 a[76:77], v242 offset:0\n\t"
    "ds_read_b64_tr_b16 a[78:79], v242 offset:2048\n\t"
    "ds_read_b64_tr_b16 a[80:81], v241 offset:0\n\t"
    "ds_read_b64_tr_b16 a[82:83], v241 offset:2048\n\t"
    "v_mfma_f32_16x16x32_bf16 v[44:47], a[116:119], a[124:127], v[44:47]\n\t"
    "ds_read_b64_tr_b16 a[112:113], v240 offset:0\n\t"
    "ds_read_b64_tr_b16 a[114:115], v240 offset:2048\n\t"
    "v_mfma_f32_16x16x32_bf16 v[72:75], a[116:119], a[128:131], v[72:75]\n\t"
    "v_mfma_f32_16x16x32_bf16 v[16:19], a[116:119], a[132:135], v[16:19]\n\t"
    "v_mfma_f32_16x16x32_bf16 v[76:79], a[116:119], a[136:139], v[76:79]\n\t"
    "v_mfma_f32_16x16x32_bf16 v[0:3], a[140:143], a[144:147], v[0:3]\n\t"
    "v_mfma_f32_16x16x32_bf16 v[4:7], a[140:143], a[152:155], v[4:7]\n\t"
    "v_mfma_f32_16x16x32_bf16 v[8:11], a[140:143], a[156:159], v[8:11]\n\t"
    "v_mfma_f32_16x16x32_bf16 v[12:15], a[140:143], a[160:163], v[12:15]\n\t"
    "v_mfma_f32_16x16x32_bf16 v[24:27], a[140:143], a[164:167], v[24:27]\n\t"
    "v_mfma_f32_16x16x32_bf16 v[32:35], a[148:151], a[144:147], v[32:35]\n\t"
    "v_mfma_f32_16x16x32_bf16 v[44:47], a[148:151], a[152:155], v[44:47]\n\t"
    "v_mfma_f32_16x16x32_bf16 v[72:75], a[148:151], a[156:159], v[72:75]\n\t"
    "v_mfma_f32_16x16x32_bf16 v[16:19], a[148:151], a[160:163], v[16:19]\n\t"
    "v_mfma_f32_16x16x32_bf16 v[76:79], a[148:151], a[164:167], v[76:79]\n\t"
    "v_mfma_f32_16x16x32_bf16 v[0:3], a[172:175], a[168:171], v[0:3]\n\t"
    "s_waitcnt vmcnt(4)\n\t"
    "ds_read_b64_tr_b16 a[116:117], v242 offset:4096\n\t"
    "ds_read_b64_tr_b16 a[118:119], v242 offset:6144\n\t"
    "ds_read_b64_tr_b16 a[140:141], v241 offset:4096\n\t"
    "ds_read_b64_tr_b16 a[142:143], v241 offset:6144\n\t"
    "v_mfma_f32_16x16x32_bf16 v[32:35], a[180:183], a[168:171], v[32:35]\n\t"
    "ds_read_b64_tr_b16 a[148:149], v240 offset:4096\n\t"
    "ds_read_b64_tr_b16 a[150:151], v240 offset:6144\n\t"
    "v_mfma_f32_16x16x32_bf16 v[0:3], a[204:207], a[196:199], v[0:3]\n\t"
    "v_mfma_f32_16x16x32_bf16 v[32:35], a[208:211], a[196:199], v[32:35]\n\t"
    "v_mfma_f32_16x16x32_bf16 v[4:7], a[172:175], a[176:179], v[4:7]\n\t"
    "v_mfma_f32_16x16x32_bf16 v[44:47], a[180:183], a[176:179], v[44:47]\n\t"
    "v_mfma_f32_16x16x32_bf16 v[4:7], a[204:207], a[200:203], v[4:7]\n\t"
    "v_mfma_f32_16x16x32_bf16 v[44:47], a[208:211], a[200:203], v[44:47]\n\t"
    "s_nop 2\n\t"
    "v_mul_f32 v0, v0, v254\n\t"
    "v_mul_f32 v1, v1, v254\n\t"
    "v_mul_f32 v2, v2, v254\n\t"
    "v_mfma_f32_16x16x32_bf16 v[8:11], a[172:175], a[184:187], v[8:11]\n\t"
    "v_mul_f32 v3, v3, v254\n\t"
    "v_cvt_pk_bf16_f32 v20, v0, v1\n\t"
    "v_cvt_pk_bf16_f32 v21, v2, v3\n\t"
    "v_mfma_f32_16x16x32_bf16 v[72:75], a[180:183], a[184:187], v[72:75]\n\t"
    "v_mul_f32 v32, v32, v254\n\t"
    "v_mul_f32 v33, v33, v254\n\t"
    "v_mul_f32 v34, v34, v254\n\t"
    "v_mfma_f32_16x16x32_bf16 v[8:11], a[204:207], a[212:215], v[8:11]\n\t"
    "v_mul_f32 v35, v35, v254\n\t"
    "v_cvt_pk_bf16_f32 v22, v32, v33\n\t"
    "v_cvt_pk_bf16_f32 v23, v34, v35\n\t"
    "v_mfma_f32_16x16x32_bf16 v[72:75], a[208:211], a[212:215], v[72:75]\n\t"
    "s_add_u32 s96, s72, 5120\n\t"
    "buffer_store_dwordx4 v[20:23], v247, %[srdP], s96 offen sc1\n\t"
    "v_mul_f32 v4, v4, v249\n\t"
    "v_mfma_f32_16x16x32_bf16 v[12:15], a[172:175], a[188:191], v[12:15]\n\t"
    "v_mul_f32 v5, v5, v249\n\t"
    "v_mul_f32 v6, v6, v249\n\t"
    "v_mul_f32 v7, v7, v249\n\t"
    "v_mfma_f32_16x16x32_bf16 v[16:19], a[180:183], a[188:191], v[16:19]\n\t"
    "v_cvt_pk_bf16_f32 v0, v4, v5\n\t"
    "v_cvt_pk_bf16_f32 v1, v6, v7\n\t"
    "v_mul_f32 v44, v44, v249\n\t"
    "v_mfma_f32_16x16x32_bf16 v[12:15], a[204:207], a[216:219], v[12:15]\n\t"
    "v_mul_f32 v45, v45, v249\n\t"
    "v_mul_f32 v46, v46, v249\n\t"
    "v_mul_f32 v47, v47, v249\n\t"
    "v_mfma_f32_16x16x32_bf16 v[16:19], a[208:211], a[216:219], v[16:19]\n\t"
    "v_cvt_pk_bf16_f32 v2, v44, v45\n\t"
    "v_cvt_pk_bf16_f32 v3, v46, v47\n\t"
    "v_mul_f32 v8, v8, v253\n\t"
    "v_mfma_f32_16x16x32_bf16 v[24:27], a[172:175], a[192:195], v[24:27]\n\t"
    "s_add_u32 s96, s72, 6144\n\t"
    "buffer_store_dwordx4 v[0:3], v247, %[srdP], s96 offen sc1\n\t"
    "v_mul_f32 v9, v9, v253\n\t"
    "v_mfma_f32_16x16x32_bf16 v[76:79], a[180:183], a[192:195], v[76:79]\n\t"
    "v_mul_f32 v10, v10, v253\n\t"
    "v_mul_f32 v11, v11, v253\n\t"
    "v_cvt_pk_bf16_f32 v0, v8, v9\n\t"
    "v_mfma_f32_16x16x32_bf16 v[24:27], a[204:207], a[220:223], v[24:27]\n\t"
    "v_cvt_pk_bf16_f32 v1, v10, v11\n\t"
    "v_mul_f32 v72, v72, v253\n\t"
    "v_mul_f32 v73, v73, v253\n\t"
    "v_mfma_f32_16x16x32_bf16 v[76:79], a[208:211], a[220:223], v[76:79]\n\t"
    "v_mul_f32 v74, v74, v253\n\t"
    "v_mul_f32 v75, v75, v253\n\t"
    "v_cvt_pk_bf16_f32 v2, v72, v73\n\t"
    "v_mfma_f32_16x16x32_bf16 v[4:7], a[224:227], a[8:11], 0\n\t"
    "v_cvt_pk_bf16_f32 v3, v74, v75\n\t"
    "v_mul_f32 v12, v12, v248\n\t"
    "v_mul_f32 v13, v13, v248\n\t"
    "v_mfma_f32_16x16x32_bf16 v[8:11], a[224:227], a[12:15], 0\n\t"
    "s_add_u32 s96, s72, 7168\n\t"
    "buffer_store_dwordx4 v[0:3], v247, %[srdP], s96 offen sc1\n\t"
    "v_mul_f32 v14, v14, v248\n\t"
    "s_nop 0\n\t"
    "v_mfma_f32_16x16x32_bf16 v[0:3], a[224:227], a[16:19], 0\n\t"
    "v_mul_f32 v15, v15, v248\n\t"
    "v_cvt_pk_bf16_f32 v20, v12, v13\n\t"
    "v_cvt_pk_bf16_f32 v21, v14, v15\n\t"
    "v_mfma_f32_16x16x32_bf16 v[12:15], a[224:227], a[20:23], 0\n\t"
    "v_mul_f32 v16, v16, v248\n\t"
    "v_mul_f32 v17, v17, v248\n\t"
    "v_mul_f32 v18, v18, v248\n\t"
    "v_mfma_f32_16x16x32_bf16 v[32:35], a[224:227], a[24:27], 0\n\t"
    "v_mul_f32 v19, v19, v248\n\t"
    "v_cvt_pk_bf16_f32 v22, v16, v17\n\t"
    "v_cvt_pk_bf16_f32 v23, v18, v19\n\t"
    "v_mfma_f32_16x16x32_bf16 v[16:19], a[228:231], a[8:11], 0\n\t"
    "s_add_u32 s96, s72, 8192\n\t"
    "buffer_store_dwordx4 v[20:23], v247, %[srdP], s96 offen sc1\n\t"
    "v_mul_f32 v24, v24, v245\n\t"
    "s_nop 0\n\t"
    "v_mfma_f32_16x16x32_bf16 v[20:23], a[228:231], a[12:15], 0\n\t"
    "v_mul_f32 v25, v25, v245\n\t"
    "v_mul_f32 v26, v26, v245\n\t"
    "v_mul_f32 v27, v27, v245\n\t"
    "v_mfma_f32_16x16x32_bf16 v[44:47], a[228:231], a[16:19], 0\n\t"
    "v_cvt_pk_bf16_f32 v48, v24, v25\n\t"
    "v_cvt_pk_bf16_f32 v49, v26, v27\n\t"
    "v_mul_f32 v76, v76, v245\n\t"
    "v_mfma_f32_16x16x32_bf16 v[24:27], a[228:231], a[20:23], 0\n\t"
    "v_mul_f32 v77, v77, v245\n\t"
    "v_mul_f32 v78, v78, v245\n\t"
    "v_mul_f32 v79, v79, v245\n\t"
    "v_mfma_f32_16x16x32_bf16 v[52:55], a[228:231], a[24:27], 0\n\t"
    "v_cvt_pk_bf16_f32 v50, v76, v77\n\t"
    "v_cvt_pk_bf16_f32 v51, v78, v79\n\t"
    "v_mfma_f32_16x16x32_bf16 v[56:59], a[232:235], a[8:11], 0\n\t"
    "s_add_u32 s96, s72, 9216\n\t"
    "buffer_store_dwordx4 v[48:51], v247, %[srdP], s96 offen sc1\n\t"
    "s_nop 1\n\t"
    "v_mfma_f32_16x16x32_bf16 v[48:51], a[232:235], a[12:15], 0\n\t"
    "v_mfma_f32_16x16x32_bf16 v[60:63], a[232:235], a[16:19], 0\n\t"
    "v_mfma_f32_16x16x32_bf16 v[64:67], a[232:235], a[20:23], 0\n\t"
    "v_mfma_f32_16x16x32_bf16 v[72:75], a[232:235], a[24:27], 0\n\t"
    "v_mfma_f32_16x16x32_bf16 v[4:7], a[236:239], a[28:31], v[4:7]\n\t"
    "v_mfma_f32_16x16x32_bf16 v[8:11], a[236:239], a[32:35], v[8:11]\n\t"
    "v_mfma_f32_16x16x32_bf16 v[0:3], a[236:239], a[36:39], v[0:3]\n\t"
    "v_mfma_f32_16x16x32_bf16 v[12:15], a[236:239], a[40:43], v[12:15]\n\t"
    "v_mfma_f32_16x16x32_bf16 v[32:35], a[236:239], a[44:47], v[32:35]\n\t"
    "v_mfma_f32_16x16x32_bf16 v[16:19], a[240:243], a[28:31], v[16:19]\n\t"
    "v_mfma_f32_16x16x32_bf16 v[20:23], a[240:243], a[32:35], v[20:23]\n\t"
    "v_mfma_f32_16x16x32_bf16 v[44:47], a[240:243], a[36:39], v[44:47]\n\t"
    "v_mfma_f32_16x16x32_bf16 v[24:27], a[240:243], a[40:43], v[24:27]\n\t"
    "v_mfma_f32_16x16x32_bf16 v[52:55], a[240:243], a[44:47], v[52:55]\n\t"
    "v_mfma_f32_16x16x32_bf16 v[56:59], a[244:247], a[28:31], v[56:59]\n\t"
    "v_mfma_f32_16x16x32_bf16 v[48:51], a[244:247], a[32:35], v[48:51]\n\t"
    "v_mfma_f32_16x16x32_bf16 v[60:63], a[244:247], a[36:39], v[60:63]\n\t"
    "v_mfma_f32_16x16x32_bf16 v[64:67], a[244:247], a[40:43], v[64:67]\n\t"
    "v_mfma_f32_16x16x32_bf16 v[72:75], a[244:247], a[44:47], v[72:75]\n\t"
    "v_mfma_f32_16x16x32_bf16 v[4:7], a[248:251], a[56:59], v[4:7]\n\t"
    "v_mfma_f32_16x16x32_bf16 v[8:11], a[248:251], a[64:67], v[8:11]\n\t"
    "v_mfma_f32_16x16x32_bf16 v[0:3], a[248:251], a[72:75], v[0:3]\n\t"
    "v_mfma_f32_16x16x32_bf16 v[12:15], a[248:251], a[84:87], v[12:15]\n\t"
    "v_mfma_f32_16x16x32_bf16 v[32:35], a[248:251], a[88:91], v[32:35]\n\t"
    "v_mfma_f32_16x16x32_bf16 v[16:19], a[252:255], a[56:59], v[16:19]\n\t"
    "v_mfma_f32_16x16x32_bf16 v[20:23], a[252:255], a[64:67], v[20:23]\n\t"
    "v_mfma_f32_16x16x32_bf16 v[44:47], a[252:255], a[72:75], v[44:47]\n\t"
    "v_mfma_f32_16x16x32_bf16 v[24:27], a[252:255], a[84:87], v[24:27]\n\t"
    "v_mfma_f32_16x16x32_bf16 v[52:55], a[252:255], a[88:91], v[52:55]\n\t"
    "v_mfma_f32_16x16x32_bf16 v[56:59], a[0:3], a[56:59], v[56:59]\n\t"
    "v_mfma_f32_16x16x32_bf16 v[48:51], a[0:3], a[64:67], v[48:51]\n\t"
    "v_mfma_f32_16x16x32_bf16 v[60:63], a[0:3], a[72:75], v[60:63]\n\t"
    "v_mfma_f32_16x16x32_bf16 v[64:67], a[0:3], a[84:87], v[64:67]\n\t"
    "v_mfma_f32_16x16x32_bf16 v[72:75], a[0:3], a[88:91], v[72:75]\n\t"
    "s_waitcnt lgkmcnt(14)\n\t"
    "v_mfma_f32_16x16x32_bf16 v[4:7], a[4:7], a[92:95], v[4:7]\n\t"
    "v_mfma_f32_16x16x32_bf16 v[8:11], a[4:7], a[96:99], v[8:11]\n\t"
    "v_mfma_f32_16x16x32_bf16 v[0:3], a[4:7], a[100:103], v[0:3]\n\t"
    "v_mfma_f32_16x16x32_bf16 v[12:15], a[4:7], a[104:107], v[12:15]\n\t"
    "v_mfma_f32_16x16x32_bf16 v[32:35], a[4:7], a[108:111], v[32:35]\n\t"
    "v_mfma_f32_16x16x32_bf16 v[16:19], v[68:71], a[92:95], v[16:19]\n\t"
    "v_mfma_f32_16x16x32_bf16 v[20:23], v[68:71], a[96:99], v[20:23]\n\t"
    "v_mfma_f32_16x16x32_bf16 v[44:47], v[68:71], a[100:103], v[44:47]\n\t"
    "v_mfma_f32_16x16x32_bf16 v[24:27], v[68:71], a[104:107], v[24:27]\n\t"
    "v_mfma_f32_16x16x32_bf16 v[52:55], v[68:71], a[108:111], v[52:55]\n\t"
    "v_mfma_f32_16x16x32_bf16 v[56:59], a[48:51], a[92:95], v[56:59]\n\t"
    "v_mfma_f32_16x16x32_bf16 v[48:51], a[48:51], a[96:99], v[48:51]\n\t"
    "v_mfma_f32_16x16x32_bf16 v[60:63], a[48:51], a[100:103], v[60:63]\n\t"
    "v_mfma_f32_16x16x32_bf16 v[64:67], a[48:51], a[104:107], v[64:67]\n\t"
    "v_mfma_f32_16x16x32_bf16 v[72:75], a[48:51], a[108:111], v[72:75]\n\t"
    "v_mfma_f32_16x16x32_bf16 v[4:7], a[52:55], a[120:123], v[4:7]\n\t"
    "v_mfma_f32_16x16x32_bf16 v[8:11], a[52:55], a[124:127], v[8:11]\n\t"
    "v_mfma_f32_16x16x32_bf16 v[0:3], a[52:55], a[128:131], v[0:3]\n\t"
    "v_mfma_f32_16x16x32_bf16 v[12:15], a[52:55], a[132:135], v[12:15]\n\t"
    "v_mfma_f32_16x16x32_bf16 v[32:35], a[52:55], a[136:139], v[32:35]\n\t"
    "v_mfma_f32_16x16x32_bf16 v[16:19], v[28:31], a[120:123], v[16:19]\n\t"
    "v_mfma_f32_16x16x32_bf16 v[20:23], v[28:31], a[124:127], v[20:23]\n\t"
    "v_mfma_f32_16x16x32_bf16 v[44:47], v[28:31], a[128:131], v[44:47]\n\t"
    "v_mfma_f32_16x16x32_bf16 v[24:27], v[28:31], a[132:135], v[24:27]\n\t"
    "v_mfma_f32_16x16x32_bf16 v[52:55], v[28:31], a[136:139], v[52:55]\n\t"
    "v_mfma_f32_16x16x32_bf16 v[56:59], a[60:63], a[120:123], v[56:59]\n\t"
    "v_mfma_f32_16x16x32_bf16 v[48:51], a[60:63], a[124:127], v[48:51]\n\t"
    "v_mfma_f32_16x16x32_bf16 v[60:63], a[60:63], a[128:131], v[60:63]\n\t"
    "v_mfma_f32_16x16x32_bf16 v[64:67], a[60:63], a[132:135], v[64:67]\n\t"
    "v_mfma_f32_16x16x32_bf16 v[72:75], a[60:63], a[136:139], v[72:75]\n\t"
    "v_mfma_f32_16x16x32_bf16 v[4:7], v[36:39], a[144:147], v[4:7]\n\t"
    "v_mfma_f32_16x16x32_bf16 v[8:11], v[36:39], a[152:155], v[8:11]\n\t"
    "v_mfma_f32_16x16x32_bf16 v[0:3], v[36:39], a[156:159], v[0:3]\n\t"
    "v_mfma_f32_16x16x32_bf16 v[12:15], v[36:39], a[160:163], v[12:15]\n\t"
    "v_mfma_f32_16x16x32_bf16 v[32:35], v[36:39], a[164:167], v[32:35]\n\t"
    "v_mfma_f32_16x16x32_bf16 v[16:19], v[40:43], a[144:147], v[16:19]\n\t"
    "v_mfma_f32_16x16x32_bf16 v[20:23], v[40:43], a[152:155], v[20:23]\n\t"
    "v_mfma_f32_16x16x32_bf16 v[44:47], v[40:43], a[156:159], v[44:47]\n\t"
    "v_mfma_f32_16x16x32_bf16 v[24:27], v[40:43], a[160:163], v[24:27]\n\t"
    "v_mfma_f32_16x16x32_bf16 v[52:55], v[40:43], a[164:167], v[52:55]\n\t"
    "s_waitcnt lgkmcnt(12)\n\t"
    "v_mfma_f32_16x16x32_bf16 v[56:59], a[68:71], a[144:147], v[56:59]\n\t"
    "v_mfma_f32_16x16x32_bf16 v[48:51], a[68:71], a[152:155], v[48:51]\n\t"
    "v_mfma_f32_16x16x32_bf16 v[60:63], a[68:71], a[156:159], v[60:63]\n\t"
    "v_mfma_f32_16x16x32_bf16 v[64:67], a[68:71], a[160:163], v[64:67]\n\t"
    "v_mfma_f32_16x16x32_bf16 v[72:75], a[68:71], a[164:167], v[72:75]\n\t"
    "s_waitcnt lgkmcnt(10)\n\t"
    "v_mfma_f32_16x16x32_bf16 v[4:7], a[76:79], a[168:171], v[4:7]\n\t"
    "s_waitcnt lgkmcnt(8)\n\t"
    "v_mfma_f32_16x16x32_bf16 v[16:19], a[80:83], a[168:171], v[16:19]\n\t"
    "s_waitcnt lgkmcnt(4)\n\t"
    "v_mfma_f32_16x16x32_bf16 v[4:7], a[116:119], a[196:199], v[4:7]\n\t"
    "s_waitcnt lgkmcnt(2)\n\t"
    "v_mfma_f32_16x16x32_bf16 v[16:19], a[140:143], a[196:199], v[16:19]\n\t"
    "v_mfma_f32_16x16x32_bf16 v[8:11], a[76:79], a[176:179], v[8:11]\n\t"
    "v_mfma_f32_16x16x32_bf16 v[20:23], a[80:83], a[176:179], v[20:23]\n\t"
    "v_mfma_f32_16x16x32_bf16 v[8:11], a[116:119], a[200:203], v[8:11]\n\t"
    "v_mfma_f32_16x16x32_bf16 v[20:23], a[140:143], a[200:203], v[20:23]\n\t"
    "s_nop 1\n\t"
    "v_mul_f32 v4, v4, v254\n\t"
    "v_mul_f32 v5, v5, v254\n\t"
    "v_mul_f32 v6, v6, v254\n\t"
    "v_mfma_f32_16x16x32_bf16 v[0:3], a[76:79], a[184:187], v[0:3]\n\t"
    "v_mul_f32 v7, v7, v254\n\t"
    "v_cvt_pk_bf16_f32 v28, v4, v5\n\t"
    "v_cvt_pk_bf16_f32 v29, v6, v7\n\t"
    "v_mfma_f32_16x16x32_bf16 v[44:47], a[80:83], a[184:187], v[44:47]\n\t"
    "v_mul_f32 v16, v16, v254\n\t"
    "v_mul_f32 v17, v17, v254\n\t"
    "v_mul_f32 v18, v18, v254\n\t"
    "v_mfma_f32_16x16x32_bf16 v[0:3], a[116:119], a[212:215], v[0:3]\n\t"
    "v_mul_f32 v19, v19, v254\n\t"
    "v_cvt_pk_bf16_f32 v30, v16, v17\n\t"
    "v_cvt_pk_bf16_f32 v31, v18, v19\n\t"
    "v_mfma_f32_16x16x32_bf16 v[44:47], a[140:143], a[212:215], v[44:47]\n\t"
    "s_add_u32 s96, s72, 10240\n\t"
    "buffer_store_dwordx4 v[28:31], v247, %[srdP], s96 offen sc1\n\t"
    "v_mul_f32 v8, v8, v249\n\t"
    "v_mfma_f32_16x16x32_bf16 v[12:15], a[76:79], a[188:191], v[12:15]\n\t"
    "v_mul_f32 v9, v9, v249\n\t"
    "v_mul_f32 v10, v10, v249\n\t"
    "v_mul_f32 v11, v11, v249\n\t"
    "v_mfma_f32_16x16x32_bf16 v[24:27], a[80:83], a[188:191], v[24:27]\n\t"
    "v_cvt_pk_bf16_f32 v4, v8, v9\n\t"
    "v_cvt_pk_bf16_f32 v5, v10, v11\n\t"
    "v_mul_f32 v20, v20, v249\n\t"
    "v_mfma_f32_16x16x32_bf16 v[12:15], a[116:119], a[216:219], v[12:15]\n\t"
    "v_mul_f32 v21, v21, v249\n\t"
    "v_mul_f32 v22, v22, v249\n\t"
    "v_mul_f32 v23, v23, v249\n\t"
    "v_mfma_f32_16x16x32_bf16 v[24:27], a[140:143], a[216:219], v[24:27]\n\t"
    "v_cvt_pk_bf16_f32 v6, v20, v21\n\t"
    "v_cvt_pk_bf16_f32 v7, v22, v23\n\t"
    "v_mul_f32 v0, v0, v253\n\t"
    "v_mfma_f32_16x16x32_bf16 v[32:35], a[76:79], a[192:195], v[32:35]\n\t"
    "s_add_u32 s96, s72, 11264\n\t"
    "buffer_store_dwordx4 v[4:7], v247, %[srdP], s96 offen sc1\n\t"
    "v_mul_f32 v1, v1, v253\n\t"
    "v_mfma_f32_16x16x32_bf16 v[52:55], a[80:83], a[192:195], v[52:55]\n\t"
    "v_mul_f32 v2, v2, v253\n\t"
    "v_mul_f32 v3, v3, v253\n\t"
    "v_cvt_pk_bf16_f32 v4, v0, v1\n\t"
    "v_mfma_f32_16x16x32_bf16 v[32:35], a[116:119], a[220:223], v[32:35]\n\t"
    "v_cvt_pk_bf16_f32 v5, v2, v3\n\t"
    "v_mul_f32 v44, v44, v253\n\t"
    "v_mul_f32 v45, v45, v253\n\t"
    "v_mfma_f32_16x16x32_bf16 v[52:55], a[140:143], a[220:223], v[52:55]\n\t"
    "v_mul_f32 v46, v46, v253\n\t"
    "v_mul_f32 v47, v47, v253\n\t"
    "v_cvt_pk_bf16_f32 v6, v44, v45\n\t"
    "v_mfma_f32_16x16x32_bf16 v[56:59], a[112:115], a[168:171], v[56:59]\n\t"
    "v_cvt_pk_bf16_f32 v7, v46, v47\n\t"
    "v_mul_f32 v12, v12, v248\n\t"
    "v_mul_f32 v13, v13, v248\n\t"
    "s_waitcnt lgkmcnt(0)\n\t"
    "v_mfma_f32_16x16x32_bf16 v[56:59], a[148:151], a[196:199], v[56:59]\n\t"
    "s_add_u32 s96, s72, 12288\n\t"
    "buffer_store_dwordx4 v[4:7], v247, %[srdP], s96 offen sc1\n\t"
    "v_mul_f32 v14, v14, v248\n\t"
    "v_mfma_f32_16x16x32_bf16 v[48:51], a[112:115], a[176:179], v[48:51]\n\t"
    "v_mul_f32 v15, v15, v248\n\t"
    "v_cvt_pk_bf16_f32 v0, v12, v13\n\t"
    "v_cvt_pk_bf16_f32 v1, v14, v15\n\t"
    "v_mfma_f32_16x16x32_bf16 v[48:51], a[148:151], a[200:203], v[48:51]\n\t"
    "v_mul_f32 v24, v24, v248\n\t"
    "v_mul_f32 v25, v25, v248\n\t"
    "v_mul_f32 v26, v26, v248\n\t"
    "v_mfma_f32_16x16x32_bf16 v[60:63], a[112:115], a[184:187], v[60:63]\n\t"
    "v_mul_f32 v27, v27, v248\n\t"
    "v_cvt_pk_bf16_f32 v2, v24, v25\n\t"
    "v_cvt_pk_bf16_f32 v3, v26, v27\n\t"
    "v_mfma_f32_16x16x32_bf16 v[60:63], a[148:151], a[212:215], v[60:63]\n\t"
    "s_add_u32 s96, s72, 13312\n\t"
    "buffer_store_dwordx4 v[0:3], v247, %[srdP], s96 offen sc1\n\t"
    "v_mul_f32 v32, v32, v245\n\t"
    "v_mfma_f32_16x16x32_bf16 v[64:67], a[112:115], a[188:191], v[64:67]\n\t"
    "v_mul_f32 v33, v33, v245\n\t"
    "v_mul_f32 v34, v34, v245\n\t"
    "v_mul_f32 v35, v35, v245\n\t"
    "v_mfma_f32_16x16x32_bf16 v[64:67], a[148:151], a[216:219], v[64:67]\n\t"
    "v_cvt_pk_bf16_f32 v0, v32, v33\n\t"
    "v_cvt_pk_bf16_f32 v1, v34, v35\n\t"
    "v_mul_f32 v52, v52, v245\n\t"
    "v_mfma_f32_16x16x32_bf16 v[72:75], a[112:115], a[192:195], v[72:75]\n\t"
    "v_mul_f32 v53, v53, v245\n\t"
    "v_mul_f32 v54, v54, v245\n\t"
    "v_mul_f32 v55, v55, v245\n\t"
    "v_mfma_f32_16x16x32_bf16 v[72:75], a[148:151], a[220:223], v[72:75]\n\t"
    "v_cvt_pk_bf16_f32 v2, v52, v53\n\t"
    "v_cvt_pk_bf16_f32 v3, v54, v55\n\t"
    "v_mul_f32 v56, v56, v254\n\t"
    "v_mul_f32 v57, v57, v254\n\t"
    "v_mul_f32 v58, v58, v254\n\t"
    "v_mul_f32 v59, v59, v254\n\t"
    "v_cvt_pk_bf16_f32 v4, v56, v57\n\t"
    "v_cvt_pk_bf16_f32 v5, v58, v59\n\t"
    "v_mul_f32 v48, v48, v249\n\t"
    "v_mul_f32 v49, v49, v249\n\t"
    "v_mul_f32 v50, v50, v249\n\t"
    "v_mul_f32 v51, v51, v249\n\t"
    "v_cvt_pk_bf16_f32 v6, v48, v49\n\t"
    "v_cvt_pk_bf16_f32 v7, v50, v51\n\t"
    "v_mul_f32 v60, v60, v253\n\t"
    "v_mul_f32 v61, v61, v253\n\t"
    "v_mul_f32 v62, v62, v253\n\t"
    "v_mul_f32 v63, v63, v253\n\t"
    "v_cvt_pk_bf16_f32 v8, v60, v61\n\t"
    "v_cvt_pk_bf16_f32 v9, v62, v63\n\t"
    "v_mul_f32 v64, v64, v248\n\t"
    "v_mul_f32 v65, v65, v248\n\t"
    "v_mul_f32 v66, v66, v248\n\t"
    "v_mul_f32 v67, v67, v248\n\t"
    "v_cvt_pk_bf16_f32 v10, v64, v65\n\t"
    "v_cvt_pk_bf16_f32 v11, v66, v67\n\t"
    "v_mul_f32 v72, v72, v245\n\t"
    "v_mul_f32 v73, v73, v245\n\t"
    "v_mul_f32 v74, v74, v245\n\t"
    "v_mul_f32 v75, v75, v245\n\t"
    "v_cvt_pk_bf16_f32 v254, v72, v73\n\t"
    "v_cvt_pk_bf16_f32 v255, v74, v75\n\t"
    "s_add_u32 s96, s72, 14336\n\t"
    "buffer_store_dwordx4 v[0:3], v247, %[srdP], s96 offen sc1\n\t"
    "s_add_u32 s96, s72, 15360\n\t"
    "buffer_store_dwordx4 v[4:7], v247, %[srdP], s96 offen sc1\n\t"
    "s_add_u32 s96, s72, 16384\n\t"
    "buffer_store_dwordx4 v[8:11], v247, %[srdP], s96 offen sc1\n\t"
    "buffer_store_dwordx2 v[254:255], v246, %[srdP], s73 offen sc1\n\t"
    : [nonce] "=&s"(nonce)
    : [srdX] "s"(srdX), [srdWq] "s"(srdWq), [srdK] "s"(srdK), [srdV] "s"(srdV), [srdWo] "s"(srdWo), [srdP] "s"(srdP), [srdF] "s"(srdF), [srdC] "s"(srdC), [srdR] "s"(srdR), [srdO] "s"(srdO),
      [lds0] "s"(lds0), [wv] "s"(wv), [hh] "s"(h), [bb] "s"(b), [jj] "s"(j)
    : "memory", "vcc", "scc", "v0", "v1", "v2", "v3", "v4", "v5", "v6", "v7", "v8", "v9", "v10", "v11", "v12", "v13", "v14", "v15", "v16", "v17", "v18", "v19", "v20", "v21", "v22", "v23", "v24", "v25", "v26", "v27", "v28", "v29", "v30", "v31", "v32", "v33", "v34", "v35", "v36", "v37", "v38", "v39", "v40", "v41", "v42", "v43", "v44", "v45", "v46", "v47", "v48", "v49", "v50", "v51", "v52", "v53", "v54", "v55", "v56", "v57", "v58", "v59", "v60", "v61", "v62", "v63", "v64", "v65", "v66", "v67", "v68", "v69", "v70", "v71", "v72", "v73", "v74", "v75", "v76", "v77", "v78", "v79", "v80", "v81", "v82", "v83", "v84", "v85", "v86", "v87", "v88", "v89", "v90", "v91", "v92", "v93", "v94", "v95", "v96", "v97", "v98", "v99", "v100", "v101", "v102", "v103", "v104", "v105", "v106", "v107", "v108", "v109", "v110", "v111", "v112", "v113", "v114", "v115", "v116", "v117", "v118", "v119", "v120", "v121", "v122", "v123", "v124", "v125", "v126", "v127", "v128", "v129", "v130", "v131", "v132", "v133", "v134", "v135", "v136", "v137", "v138", "v139", "v140", "v141", "v142", "v143", "v144", "v145", "v146", "v147", "v148", "v149", "v150", "v151", "v152", "v153", "v154", "v155", "v156", "v157", "v158", "v159", "v160", "v161", "v162", "v163", "v164", "v165", "v166", "v167", "v168", "v169", "v170", "v171", "v172", "v173", "v174", "v175", "v176", "v177", "v178", "v179", "v180", "v181", "v182", "v183", "v184", "v185", "v186", "v187", "v188", "v189", "v190", "v191", "v192", "v193", "v194", "v195", "v196", "v197", "v198", "v199", "v200", "v201", "v202", "v203", "v204", "v205", "v206", "v207", "v208", "v209", "v210", "v211", "v212", "v213", "v214", "v215", "v216", "v217", "v218", "v219", "v220", "v221", "v222", "v223", "v224", "v225", "v226", "v227", "v228", "v229", "v230", "v231", "v232", "v233", "v234", "v235", "v236", "v237", "v238", "v239", "v240", "v241", "v242", "v243", "v244", "v245", "v246", "v247", "v248", "v249", "v250", "v251", "v252", "v253", "v254", "v255", "a0", "a1", "a2", "a3", "a4", "a5", "a6", "a7", "a8", "a9", "a10", "a11", "a12", "a13", "a14", "a15", "a16", "a17", "a18", "a19", "a20", "a21", "a22", "a23", "a24", "a25", "a26", "a27", "a28", "a29", "a30", "a31", "a32", "a33", "a34", "a35", "a36", "a37", "a38", "a39", "a40", "a41", "a42", "a43", "a44", "a45", "a46", "a47", "a48", "a49", "a50", "a51", "a52", "a53", "a54", "a55", "a56", "a57", "a58", "a59", "a60", "a61", "a62", "a63", "a64", "a65", "a66", "a67", "a68", "a69", "a70", "a71", "a72", "a73", "a74", "a75", "a76", "a77", "a78", "a79", "a80", "a81", "a82", "a83", "a84", "a85", "a86", "a87", "a88", "a89", "a90", "a91", "a92", "a93", "a94", "a95", "a96", "a97", "a98", "a99", "a100", "a101", "a102", "a103", "a104", "a105", "a106", "a107", "a108", "a109", "a110", "a111", "a112", "a113", "a114", "a115", "a116", "a117", "a118", "a119", "a120", "a121", "a122", "a123", "a124", "a125", "a126", "a127", "a128", "a129", "a130", "a131", "a132", "a133", "a134", "a135", "a136", "a137", "a138", "a139", "a140", "a141", "a142", "a143", "a144", "a145", "a146", "a147", "a148", "a149", "a150", "a151", "a152", "a153", "a154", "a155", "a156", "a157", "a158", "a159", "a160", "a161", "a162", "a163", "a164", "a165", "a166", "a167", "a168", "a169", "a170", "a171", "a172", "a173", "a174", "a175", "a176", "a177", "a178", "a179", "a180", "a181", "a182", "a183", "a184", "a185", "a186", "a187", "a188", "a189", "a190", "a191", "a192", "a193", "a194", "a195", "a196", "a197", "a198", "a199", "a200", "a201", "a202", "a203", "a204", "a205", "a206", "a207", "a208", "a209", "a210", "a211", "a212", "a213", "a214", "a215", "a216", "a217", "a218", "a219", "a220", "a221", "a222", "a223", "a224", "a225", "a226", "a227", "a228", "a229", "a230", "a231", "a232", "a233", "a234", "a235", "a236", "a237", "a238", "a239", "a240", "a241", "a242", "a243", "a244", "a245", "a246", "a247", "a248", "a249", "a250", "a251", "a252", "a253", "a254", "a255", "s64", "s65", "s66", "s67", "s68", "s69", "s70", "s71", "s72", "s73", "s74", "s75", "s76", "s77", "s78", "s79", "s80", "s81", "s82", "s83", "s84", "s85", "s86", "s87", "s88", "s89", "s90", "s91", "s92", "s93", "s94", "s95", "s96", "s97", "s98", "s99");
  (void)nonce;
  const unsigned lane = lane_id(), l15 = lane & 15u;
  unsigned fl = __builtin_amdgcn_raw_buffer_load_b32(srdF, (h + 1u) * 128u + l15 * 4u, 0, 16);
  unsigned nv = __builtin_amdgcn_raw_buffer_load_b32(srdF, l15 * 4u, 0, 16);
  asm volatile("s_waitcnt vmcnt(0)" : "+v"(fl), "+v"(nv) :: "memory");
  __builtin_amdgcn_s_barrier();
  if (wv == 0u) __builtin_amdgcn_raw_buffer_store_b32(nv, srdF, (l15 + 1u) * 128u + h * 4u, 0, 16);
  {
    const unsigned mine = __builtin_amdgcn_readlane(nv, h);
    const unsigned c0 = h * 280u + wv * 64u + lane;
    const unsigned d1 = wv * 24u + lane, a1 = lane < 24u ? (h * 280u + 256u) * 16u + d1 * 4u : 0x7ffffff0u;
    u32x4 v[16]; unsigned e[16];
#define GQA_FETCH(rdy) do { __builtin_amdgcn_fence(__ATOMIC_ACQUIRE, "wavefront"); \
      _Pragma("unroll") for (int hh = 0; hh < 16; ++hh) if (((rdy) >> hh) & 1u) { \
          v[hh] = __builtin_amdgcn_raw_buffer_load_b128(srdR, c0 * 16u, 128 + hh * PART_BYTES, 16); \
          e[hh] = __builtin_amdgcn_raw_buffer_load_b32(srdR, a1, 128 + hh * PART_BYTES, 16); } } while (0)
    unsigned done = (((unsigned)__builtin_amdgcn_ballot_w64(fl == mine) & 0xffffu) | (1u << h)), spins = 0;
    GQA_FETCH(done);
    __builtin_amdgcn_sched_barrier(0);
    unsigned o0, o1, p0, p1;
    chunk_pos(c0, o0, o1); chunk_pos(h * 280u + 256u + (d1 >> 2), p0, p1);
    unsigned pe = lane < 24u ? ((d1 & 2u) ? p1 : p0) + (d1 & 1u) * 4u : 0x7ffffff0u;
    asm volatile("" : "+v"(o0), "+v"(o1), "+v"(pe));
    while (done != 0xffffu && ++spins <= 200000u) {
      fl = __builtin_amdgcn_raw_buffer_load_b32(srdF, (h + 1u) * 128u + l15 * 4u, 0, 16);
      const unsigned rdy = (unsigned)__builtin_amdgcn_ballot_w64(fl == mine) & 0xffffu & ~done;
      GQA_FETCH(rdy);
      done |= rdy;
    }
    unsigned nv2 = 0;
    if (wv == 0u) nv2 = __builtin_amdgcn_raw_buffer_load_b32(srdF, l15 * 4u, 0, 16);
    sum_store(v, o0, o1, srdO);
    if (wv == 0u) __builtin_amdgcn_raw_buffer_store_b32(nv2, srdF, (l15 + 1u) * 128u + h * 4u, 0, 16);
    unsigned re; GQA_SUM16(re, e[hh]);
    __builtin_amdgcn_raw_buffer_store_b32(re, srdO, pe, 0, 0);
  }
  asm volatile(".globl gqa_main_end\n\t.hidden gqa_main_end\n\tgqa_main_end:");
}


extern "C" void kernel_launch(void* const* d_in, const int* in_sizes, int n_in, void* d_out, int out_size, void* d_ws, size_t ws_size, hipStream_t stream) {
  (void)in_sizes; (void)n_in; (void)out_size; (void)ws_size;
  (void)hipFuncSetAttribute((const void*)gqa_main, hipFuncAttributeMaxDynamicSharedMemorySize, 163840);
  gqa_main<<<256, 256, 163840, stream>>>((const char*)d_in[0], (const char*)d_in[1], (const char*)d_in[2], (const char*)d_in[3], (const char*)d_in[4], (char*)d_ws, (char*)d_out);
}
```
